```python
import torch

def _softmax(x, axis=-1):
    return torch.softmax(x, dim=axis)

def _gelu(x):
    return torch.nn.functional.gelu(x, approximate='tanh')

def _silu(x):
    return torch.nn.functional.silu(x)

def _sigmoid(x):
    return torch.sigmoid(x)

def _layernorm(x, axis=-1, eps=1e-05):
    m = x.mean(dim=axis, keepdim=True)
    v = x.var(dim=axis, keepdim=True, unbiased=False)
    return (x - m) / torch.sqrt(v + eps)

def _scatter_add(tgt, idx, src, axis=0):
    return tgt.clone().index_add_(axis, idx, src)

def _segment_sum(src, seg, n):
    out = torch.zeros((n,) + src.shape[1:], dtype=src.dtype)
    return out.index_add_(0, seg, src)

def _pad(x, pw):
    flat = tuple((v for pair in reversed(pw) for v in pair))
    return torch.nn.functional.pad(x, flat)
kernel_name = 'interleave_layers__memory_aug_recurrent_mem_tr__grouped_kv_mqa__prompt'

def setup_inputs():

    _w = lambda a: (lambda t: t.to(torch.bfloat16) if t.is_floating_point() else t)(torch.as_tensor(a))

    def _A_setup():
        B, S, Dm = (1, 2048, 448)
        H, Dh = (16, 256)
        M, seg = (8, 32)
        seg = min(seg, S)
        S = S // seg * seg
        return {'x': _w((lambda *s: torch.randn(*s, dtype=torch.bfloat16) * 0.05)(B, S, Dm)), 'mem0': _w((lambda *s: torch.randn(*s, dtype=torch.bfloat16) * 0.2)(B, M, Dm)), 'Wq': _w((lambda *s: torch.randn(*s, dtype=torch.bfloat16) * 1.6)(Dm, H, Dh) * Dm ** (-0.5)), 'Wk': _w((lambda *s: torch.randn(*s, dtype=torch.bfloat16) * 1.6)(Dm, H, Dh) * Dm ** (-0.5)), 'Wv': _w((lambda *s: torch.randn(*s, dtype=torch.bfloat16) * 0.2)(Dm, H, Dh) * Dm ** (-0.5)), 'Wo': _w((lambda *s: torch.randn(*s, dtype=torch.bfloat16) * 0.2)(H, Dh, Dm) * (H * Dh) ** (-0.5)), 'seg_len': seg}
    _A_in = _A_setup()

    def _B_setup():
        B, S, D, H = (1, 2048, 448, 16)
        Dh = D // H
        return {'x': _w((lambda *s: torch.randn(*s, dtype=torch.bfloat16) * 0.05)(B, S, D)), 'wq': _w((lambda *s: torch.randn(*s, dtype=torch.bfloat16) * 0.05)(D, H * Dh)), 'wk': _w((lambda *s: torch.randn(*s, dtype=torch.bfloat16) * 0.05)(D, Dh)), 'wv': _w((lambda *s: torch.randn(*s, dtype=torch.bfloat16) * 0.05)(D, Dh)), 'wo': _w((lambda *s: torch.randn(*s, dtype=torch.bfloat16) * 0.05)(H * Dh, D))}
    _B_in = _B_setup()
    return {'A_x': _A_in['x'], 'A_mem0': _A_in['mem0'], 'A_Wq': _A_in['Wq'], 'A_Wk': _A_in['Wk'], 'A_Wv': _A_in['Wv'], 'A_Wo': _A_in['Wo'], 'A_seg_len': _A_in['seg_len'], 'B_x': _B_in['x'], 'B_wq': _B_in['wq'], 'B_wk': _B_in['wk'], 'B_wv': _B_in['wv'], 'B_wo': _B_in['wo']}

def reference(A_x, A_mem0, A_Wq, A_Wk, A_Wv, A_Wo, A_seg_len, B_x, B_wq, B_wk, B_wv, B_wo):

    def _A_ref(x, mem0, Wq, Wk, Wv, Wo, seg_len):
        B, S, Dm = x.shape
        H, Dh = (Wq.shape[1], Wq.shape[2])
        M = mem0.shape[1]
        scale = Dh ** (-0.5)
        n_seg = S // seg_len
        L = M + seg_len
        idx = torch.arange(L)
        mask = (idx[:, None] < M) | (idx[:, None] >= idx[None, :])
        mem = mem0
        outs = []
        for s in range(n_seg):
            xs = x[:, s * seg_len:(s + 1) * seg_len, :].clone()
            h = torch.concatenate([mem, xs], axis=1)
            q = torch.einsum('btd,dhe->bhte', h, Wq)
            k = torch.einsum('btd,dhe->bhte', h, Wk)
            v = torch.einsum('btd,dhe->bhte', h, Wv)
            scores = torch.einsum('bhqe,bhke->bhqk', q, k) * scale
            scores = torch.where(mask[None, None], scores, -1000000000.0)
            p = _softmax(scores, axis=-1)
            o = torch.einsum('bhqk,bhke->bhqe', p, v)
            o = torch.einsum('bhte,hed->btd', o, Wo)
            mem = o[:, :M, :].clone()
            outs.append(o[:, M:, :])
        return torch.concatenate(outs, axis=1)

    def _B_ref(x, wq, wk, wv, wo):
        B, S, D = x.shape
        H = 16
        Dh = D // H
        q = (lambda x, *d: x.permute(*d))((x @ wq).reshape(B, S, H, Dh), 0, 2, 1, 3)
        k = x @ wk
        v = x @ wv
        scores = torch.einsum('bhqd,bkd->bhqk', q, k) * Dh ** (-0.5)
        p = _softmax(scores, axis=-1)
        o = torch.einsum('bhqk,bkd->bhqd', p, v)
        return (lambda x, *d: x.permute(*d))(o, 0, 2, 1, 3).reshape(B, S, H * Dh) @ wo
    x = A_x
    x = x + _A_ref(_layernorm(x, axis=-1), A_mem0, A_Wq, A_Wk, A_Wv, A_Wo, A_seg_len)
    x = x + _B_ref(_layernorm(x, axis=-1), B_wq, B_wk, B_wv, B_wo)
    x = x + _A_ref(_layernorm(x, axis=-1), A_mem0, A_Wq, A_Wk, A_Wv, A_Wo, A_seg_len)
    x = x + _B_ref(_layernorm(x, axis=-1), B_wq, B_wk, B_wv, B_wo)
    return x
```

```cpp
#include <hip/hip_runtime.h>
#include <hip/hip_bf16.h>
#include <stdint.h>

#pragma float_control(precise, off)

typedef unsigned short u16;
typedef __bf16 bf8 __attribute__((ext_vector_type(8)));
typedef __bf16 bf16v __attribute__((ext_vector_type(16)));
typedef __bf16 bf4 __attribute__((ext_vector_type(4)));
typedef __bf16 bf2 __attribute__((ext_vector_type(2)));
typedef float f4 __attribute__((ext_vector_type(4)));
typedef float f16v __attribute__((ext_vector_type(16)));
typedef unsigned u4 __attribute__((ext_vector_type(4)));
typedef unsigned u2 __attribute__((ext_vector_type(2)));
#define LAS __attribute__((address_space(3)))
#define DEV __device__ __forceinline__

constexpr int SEQ = 2048, DM = 448, NH = 16, DH = 256, NSEG = 64;
constexpr float LOG2E = 1.4426950408889634f;

constexpr size_t MB = 1u << 20;
constexpr size_t OFF_HOP = 0;
constexpr size_t OFF_WAF = 8 * MB;
constexpr size_t OFF_WBF = OFF_WAF + 6422528;
constexpr size_t OFF_XN = OFF_WBF + 6422528;
constexpr size_t OFF_XP = OFF_XN + 1835008;
constexpr size_t OFF_KS = OFF_XP + 1835008;
constexpr size_t OFF_VS = OFF_KS + 29360128;
constexpr size_t OFF_KM = OFF_VS + 29360128;
constexpr size_t OFF_VM = OFF_KM + 7340032;
constexpr size_t OFF_PS = OFF_VM + 7340032;
constexpr size_t OFF_PM = OFF_PS + 2097152;
constexpr size_t OFF_X32 = OFF_PM + 524288;
constexpr size_t OFF_WQF = OFF_X32 + 3670016;
constexpr size_t OFF_WOF = OFF_WQF + 458752;
constexpr size_t OFF_BQ = OFF_WOF + 401408;
constexpr size_t OFF_BK = OFF_BQ + 2097152;
constexpr size_t OFF_BV = OFF_BK + 131072;
constexpr size_t OFF_BO = OFF_BV + 131072;
constexpr size_t OFF_BS = OFF_BO + 3670016;
constexpr size_t OFF_CTL = OFF_BS + 524288;
constexpr size_t CTL_BYTES = 262144;
constexpr size_t WS_END = OFF_CTL + CTL_BYTES;
enum { F12K = 0, F12V = 1, FKM = 2, FVM = 3, FPS = 4, NFLAG = 5 };
constexpr unsigned FLAG0 = 65536;
__host__ __device__ inline unsigned flag_off(int layer, int type, int s) { return (unsigned)OFF_CTL + FLAG0 + (unsigned)((layer * NFLAG + type) * 64 + s) * 128u; }

#define KST(k, i)
constexpr unsigned SENT = 0xFFFFFFFFu;
constexpr unsigned HOP_STEP = 49152;

DEV unsigned pk(float a, float b) { bf2 v = {(__bf16)a, (__bf16)b}; return __builtin_bit_cast(unsigned, v); }
DEV float bflo(unsigned w) { return __uint_as_float(w << 16); }
DEV float bfhi(unsigned w) { return __uint_as_float(w & 0xffff0000u); }
DEV bf8 asbf8(u4 v) { return __builtin_bit_cast(bf8, v); }
DEV f4 mfma16(bf8 a, bf8 b, f4 c) { return __builtin_amdgcn_mfma_f32_16x16x32_bf16(a, b, c, 0, 0, 0); }
DEV f16v mfma32(bf8 a, bf8 b, f16v c) { return __builtin_amdgcn_mfma_f32_32x32x16_bf16(a, b, c, 0, 0, 0); }
struct W16 { u4 lo, hi; };
DEV bf16v join16(u4 lo, u4 hi) {
    typedef unsigned u8v __attribute__((ext_vector_type(8)));
    u8v t = {lo.x, lo.y, lo.z, lo.w, hi.x, hi.y, hi.z, hi.w};
    return __builtin_bit_cast(bf16v, t);
}
DEV float swap32_partner_max(float v) {
    auto r = __builtin_amdgcn_permlane32_swap(__float_as_uint(v), __float_as_uint(v), false, false);
    return fmaxf(__uint_as_float(r[0]), __uint_as_float(r[1]));
}
DEV float swap32_partner_add(float v) {
    auto r = __builtin_amdgcn_permlane32_swap(__float_as_uint(v), __float_as_uint(v), false, false);
    return __uint_as_float(r[0]) + __uint_as_float(r[1]);
}
DEV float swap16_partner_max(float v) {
    auto r = __builtin_amdgcn_permlane16_swap(__float_as_uint(v), __float_as_uint(v), false, false);
    return fmaxf(__uint_as_float(r[0]), __uint_as_float(r[1]));
}
DEV float swap16_partner_add(float v) {
    auto r = __builtin_amdgcn_permlane16_swap(__float_as_uint(v), __float_as_uint(v), false, false);
    return __uint_as_float(r[0]) + __uint_as_float(r[1]);
}
DEV float ex2(float x) { return __builtin_amdgcn_exp2f(x); }
DEV void lds_barrier() { asm volatile("s_waitcnt lgkmcnt(0)" ::: "memory"); __builtin_amdgcn_s_barrier(); asm volatile("" ::: "memory"); }
DEV unsigned umax3(unsigned a, unsigned b, unsigned c) { unsigned t = a > b ? a : b; return t > c ? t : c; }
DEV unsigned umax4(u4 v) { return umax3(umax3(v.x, v.y, v.z), v.w, 0u); }
DEV unsigned nosent(unsigned w) { return w < 0xFFFFFFFEu ? w : 0xFFFFFFFEu; }

__host__ __device__ inline int permb(int t, int R, int gB, int j) {
    return 64 * t + 16 * (2 * R + (gB >> 1)) + 8 * ((j >> 1) & 1) + 2 * (2 * (gB & 1) + (j >> 2)) + (j & 1);
}

DEV void glds16(const void* gsrc, unsigned lds_dst) {
    unsigned keep;
    asm volatile("s_mov_b32 %0, m0\n\ts_mov_b32 m0, %2\n\ts_nop 0\n\tglobal_load_lds_dwordx4 %1, off\n\ts_mov_b32 m0, %0" : "=&s"(keep) : "v"(gsrc), "s"(lds_dst) : "memory");
}
DEV u4 lds_ld(unsigned addr) { return *(LAS const u4*)(size_t)addr; }
DEV void lds_st(unsigned addr, u4 v) { *(LAS u4*)(size_t)addr = v; }

template <int MAT>
DEV void prep_w_body(int h, int t, const u16* __restrict__ Wa, const u16* __restrict__ Wb, char* __restrict__ ws) {
    const int tid = threadIdx.x, lane = tid & 63, w = __builtin_amdgcn_readfirstlane(tid >> 6);
    const int l15 = lane & 15, kg = lane >> 4;
    const int prow = lane >> 5, ppos = lane & 31;
    auto stage_rows = [&](const u16* base, int row0, int rowstride, unsigned dst) {
#pragma unroll
        for (int p = 0; p < 8; ++p) {
            const int r = 2 * (8 * w + p) + prow;
            glds16(base + ((size_t)(row0 + r) * NH + h) * DH + ((ppos ^ (r & 15)) << 3), dst + (8 * w + p) * 1024);
        }
    };
    const char* slab_src = (const char*)(Wb + ((size_t)h * DH + (lane >> 3)) * DM + (lane & 7) * 8);
    auto stage = [&](int i) {
        if (MAT == 0) stage_rows(Wb, 64 * i, 1, (i & 1) * 32768);
        else {
#pragma unroll
            for (int p = 0; p < 8; ++p) glds16(slab_src + ((size_t)(8 * (8 * w + p)) * DM + 64 * i) * 2, (i & 1) * 32768 + (8 * w + p) * 1024);
        }
    };
    stage_rows(Wa, 64 * t, 1, 32768);
    stage(0);
    asm volatile("s_waitcnt vmcnt(8)" ::: "memory");
    __builtin_amdgcn_s_barrier();
    asm volatile("" ::: "memory");
    bf8 av[2][2][8];
#pragma unroll
    for (int R = 0; R < 2; ++R)
#pragma unroll
        for (int u = 0; u < 2; ++u) {
            const int row = permb(0, R, l15 >> 2, 4 * u + (l15 & 3));
#pragma unroll
            for (int ke = 0; ke < 8; ++ke) av[R][u][ke] = asbf8(lds_ld(32768 + row * 512 + (((4 * ke + kg) ^ (row & 15)) << 4)));
        }
    asm volatile("s_waitcnt lgkmcnt(0)" ::: "memory");
    const float sc = MAT ? 1.0f : LOG2E / 16.0f;
    const unsigned tra = (8 * kg + (l15 >> 2)) * 128 + (16 * w + 4 * (l15 & 3)) * 2;
#pragma unroll 1
    for (int i = 0; i < 7; ++i) {
        const int T = 4 * i + w;
        bf8 bv[8];
        asm volatile("s_waitcnt vmcnt(0)" ::: "memory");
        __builtin_amdgcn_s_barrier();
        asm volatile("" ::: "memory");
        if (i < 6) stage(i + 1);
        if (MAT == 0) {
            const int r = 16 * w + l15;
#pragma unroll
            for (int ke = 0; ke < 8; ++ke) bv[ke] = asbf8(lds_ld((i & 1) * 32768 + r * 512 + (((4 * ke + kg) ^ (r & 15)) << 4)));
        } else {
#pragma unroll
            for (int ke = 0; ke < 8; ++ke) {
                const unsigned ad = (i & 1) * 32768 + ke * 4096 + tra;
                bf4 lo = __builtin_amdgcn_ds_read_tr16_b64_v4bf16((LAS bf4*)(size_t)ad);
                bf4 hi = __builtin_amdgcn_ds_read_tr16_b64_v4bf16((LAS bf4*)(size_t)(ad + 512));
                bv[ke] = bf8{lo[0], lo[1], lo[2], lo[3], hi[0], hi[1], hi[2], hi[3]};
            }
        }
        f4 acc[2][2];
#pragma unroll
        for (int R = 0; R < 2; ++R)
#pragma unroll
            for (int u = 0; u < 2; ++u) acc[R][u] = f4{0, 0, 0, 0};
#pragma unroll
        for (int ke = 0; ke < 8; ++ke)
#pragma unroll
            for (int R = 0; R < 2; ++R)
#pragma unroll
                for (int u = 0; u < 2; ++u) acc[R][u] = mfma16(av[R][u][ke], bv[ke], acc[R][u]);
#pragma unroll
        for (int R = 0; R < 2; ++R) {
            u4 v;
            v.x = pk(acc[R][0][0] * sc, acc[R][0][1] * sc); v.y = pk(acc[R][0][2] * sc, acc[R][0][3] * sc);
            v.z = pk(acc[R][1][0] * sc, acc[R][1][1] * sc); v.w = pk(acc[R][1][2] * sc, acc[R][1][3] * sc);
            const size_t fr = MAT ? (((size_t)T * 16 + h) * 7 + t) * 2 + R : (((size_t)h * 28 + T) * 7 + t) * 2 + R;
            *(u4*)(ws + (MAT ? OFF_WBF : OFF_WAF) + fr * 1024 + lane * 16) = v;
        }
        asm volatile("s_waitcnt lgkmcnt(0)" ::: "memory");
    }
}

DEV void prep_misc_body(int gid, int nthr, const u16* __restrict__ mem0, const u16* __restrict__ wq, const u16* __restrict__ wk,
                        const u16* __restrict__ wv, const u16* __restrict__ wo, char* __restrict__ ws) {
    constexpr unsigned PAGE_USED = 32 * HOP_STEP + 32768;
    for (int i = gid; i < 4 * (int)(PAGE_USED / 16); i += nthr) {
        const unsigned page = (unsigned)i / (PAGE_USED / 16), inp = ((unsigned)i % (PAGE_USED / 16)) * 16;
        u4 v = {SENT, SENT, SENT, SENT};
        if ((page & 1) == 0 && inp < 16384) {
            const unsigned o = inp & 8191;
            if (o < 7168) {
                const int l = (o >> 4) & 63, tt = o >> 10;
                const int tok = l & 7, col = 64 * tt + 16 * (l >> 4) + 8 * ((l >> 3) & 1);
                v = *(const u4*)(mem0 + tok * DM + col);
                v.x = nosent(v.x); v.y = nosent(v.y); v.z = nosent(v.z); v.w = nosent(v.w);
            }
        }
        *(u4*)(ws + OFF_HOP + (size_t)page * (2 * MB) + inp) = v;
    }
    for (int i = gid; i < (int)(CTL_BYTES / 16); i += nthr) {
        u4 z = u4{0, 0, 0, 0};
        *(u4*)(ws + OFF_CTL + (size_t)i * 16) = z;
    }
    for (int i = gid; i < 60 * 14 * 64; i += nthr) {
        const int lane = i & 63, ks = (i >> 6) % 14, T = (i >> 6) / 14;
        const int n = lane & 15, kg = lane >> 4;
        u16 v[8];
#pragma unroll
        for (int j = 0; j < 8; ++j) {
            const int k = 32 * ks + 8 * kg + j;
            u16 x;
            if (T < 28) x = wq[k * 448 + 16 * T + n];
            else if (T < 32) { const int c = 16 * ((T - 28) & 1) + n; const u16* wsrc = T < 30 ? wk : wv; x = c < 28 ? wsrc[k * 28 + c] : (u16)0; }
            else x = wo[k * 448 + 16 * (T - 32) + n];
            v[j] = x;
        }
        u4 o = {v[0] | ((unsigned)v[1] << 16), v[2] | ((unsigned)v[3] << 16), v[4] | ((unsigned)v[5] << 16), v[6] | ((unsigned)v[7] << 16)};
        *(u4*)(ws + OFF_WQF + (size_t)i * 16) = o;
    }
}

constexpr int XLD = DM + 4;
DEV float row16_sum(float v) {
    v += __uint_as_float(__builtin_amdgcn_update_dpp(0u, __float_as_uint(v), 0x128  , 0xf, 0xf, false));
    v += __uint_as_float(__builtin_amdgcn_update_dpp(0u, __float_as_uint(v), 0x124  , 0xf, 0xf, false));
    v += __uint_as_float(__builtin_amdgcn_update_dpp(0u, __float_as_uint(v), 0x122  , 0xf, 0xf, false));
    v += __uint_as_float(__builtin_amdgcn_update_dpp(0u, __float_as_uint(v), 0x121  , 0xf, 0xf, false));
    return v;
}
DEV void ln_rows_to_frags(const float* __restrict__ xr, u16* __restrict__ xb, int tile16, char* __restrict__ ws, int tid) {
    const int lane = tid & 63, w = tid >> 6;
    {
        const int r = w * 4 + (lane >> 4), j = lane & 15;
        f4 v[7];
#pragma unroll
        for (int i = 0; i < 7; ++i) v[i] = *(const f4*)(xr + r * XLD + j * 28 + 4 * i);
        float s = 0;
#pragma unroll
        for (int i = 0; i < 7; ++i) s += (v[i][0] + v[i][1]) + (v[i][2] + v[i][3]);
        const float mean = row16_sum(s) * (1.0f / DM);
        float q = 0;
#pragma unroll
        for (int i = 0; i < 7; ++i) { v[i] -= mean; q += (v[i][0] * v[i][0] + v[i][1] * v[i][1]) + (v[i][2] * v[i][2] + v[i][3] * v[i][3]); }
        const float rstd = 1.0f / sqrtf(row16_sum(q) * (1.0f / DM) + 1e-5f);
#pragma unroll
        for (int i = 0; i < 7; ++i) *(u2*)(xb + r * 456 + j * 28 + 4 * i) = u2{pk(v[i][0] * rstd, v[i][1] * rstd), pk(v[i][2] * rstd, v[i][3] * rstd)};
    }
    __syncthreads();
    if (ws) {
        const int s = tile16 >> 1, kt = tile16 & 1;
        char* xn = ws + OFF_XN + (size_t)(s * 2 + kt) * 14336;
        char* xp = ws + OFF_XP + (size_t)(s * 2 + kt) * 14336;
        const int row = lane & 15, kg = lane >> 4;
        for (int f = w; f < 14; f += 4) {
            *(u4*)(xn + f * 1024 + lane * 16) = *(const u4*)(xb + row * 456 + 32 * f + 8 * kg);
            const int t = f >> 1, R = f & 1;
            const unsigned* base = (const unsigned*)(xb + row * 456 + 64 * t + 16 * (2 * R + (kg >> 1)));
            const int s2 = 2 * (kg & 1);
            u4 v = {base[s2], base[4 + s2], base[s2 + 1], base[5 + s2]};
            *(u4*)(xp + f * 1024 + lane * 16) = v;
        }
    }
}

DEV void ln0_body(int tile, const u16* __restrict__ x, char* __restrict__ ws, float* xr, u16* xb) {
    const int tid = threadIdx.x;
    float* x32 = (float*)(ws + OFF_X32);
    for (int i = tid; i < 16 * 56; i += 256) {
        const int r = i / 56, c = (i % 56) * 8;
        u4 v = *(const u4*)(x + (size_t)(tile * 16 + r) * DM + c);
        f4 a = {bflo(v.x), bfhi(v.x), bflo(v.y), bfhi(v.y)}, b = {bflo(v.z), bfhi(v.z), bflo(v.w), bfhi(v.w)};
        *(f4*)(xr + r * XLD + c) = a; *(f4*)(xr + r * XLD + c + 4) = b;
        *(f4*)(x32 + (size_t)(tile * 16 + r) * DM + c) = a; *(f4*)(x32 + (size_t)(tile * 16 + r) * DM + c + 4) = b;
    }
    __syncthreads();
    ln_rows_to_frags(xr, xb, tile, ws, tid);
}
struct PrepArgs { const u16 *x, *mem0, *Wq, *Wk, *Wv, *Wo, *wq, *wk, *wv, *wo; char* ws; };
__global__ void __launch_bounds__(256, 2) k_prep(PrepArgs p) {
    extern __shared__ __attribute__((aligned(16))) char dyn[];
    const int b = blockIdx.x;
    char* ws = p.ws;
    if (b < 224) {
        const int k = b >> 3, r = k % 14, h = 2 * (b & 7) + r / 7, t = r % 7;
        if (k < 14) prep_w_body<1>(h, t, p.Wv, p.Wo, p.ws); else prep_w_body<0>(h, t, p.Wk, p.Wq, p.ws);
    }
    else if (b < 352) ln0_body(b - 224, p.x, p.ws, (float*)dyn, (u16*)(dyn + 16 * XLD * 4));
    else prep_misc_body((b - 352) * 256 + threadIdx.x, (gridDim.x - 352) * 256, p.mem0, p.wq, p.wk, p.wv, p.wo, p.ws);
}

struct RecArgs {
    char* ws;
    int layer;
    int pol;
    int xs, xv;
    int dA, dB1, dB2;
    int pad;
};
DEV void delay16(int n) { for (int i = 0; i < n; ++i) asm volatile("s_nop 3" ::: "memory"); }
constexpr int SPIN_CAP = 1 << 16;
#define COUNT_PASS(x)
template <bool B> struct BoolC { static constexpr bool value = B; };
#define ST_DECL
#define STAMP(k)
#define ST_STEP(slot, cond)
#define ST_OUT(slot)
#define TL(slot)
#define TLMAX(slot)
typedef __amdgpu_buffer_rsrc_t rsrc_t;
DEV u4 ld_sc1(rsrc_t rs, unsigned voff, unsigned soff) { return __builtin_amdgcn_raw_buffer_load_b128(rs, voff, soff, 16); }
DEV void st_sc1(u4 v, rsrc_t rs, unsigned voff, unsigned soff) { __builtin_amdgcn_raw_buffer_store_b128(v, rs, voff, soff, 16); }
#define SMF(acc, a, w, idx) asm volatile("v_smfmac_f32_16x16x64_bf16 %0, %1, %2, %3" : "+v"(acc) : "v"(a), "a"(w), "v"(idx))
#define MF0(d, a, b) asm volatile("v_mfma_f32_16x16x32_bf16 %0, %1, %2, 0" : "=&v"(d) : "v"(a), "v"(b))
#define MF0N(d, a, b) asm volatile("s_nop 1\n\tv_mfma_f32_16x16x32_bf16 %0, %1, %2, 0" : "=&v"(d) : "v"(a), "v"(b))
#define MF(d, a, b) asm volatile("v_mfma_f32_16x16x32_bf16 %0, %1, %2, %0" : "+v"(d) : "v"(a), "v"(b))
#define MF0A(d, a, b) asm volatile("v_mfma_f32_16x16x32_bf16 %0, %1, %2, 0" : "=&v"(d) : "a"(a), "v"(b))
#define MFA(d, a, b) asm volatile("v_mfma_f32_16x16x32_bf16 %0, %1, %2, %0" : "+v"(d) : "a"(a), "v"(b))
#define MF0B(d, a, b) asm volatile("v_mfma_f32_16x16x32_bf16 %0, %1, %2, 0" : "=&v"(d) : "v"(a), "a"(b))
#define MFB(d, a, b) asm volatile("v_mfma_f32_16x16x32_bf16 %0, %1, %2, %0" : "+v"(d) : "v"(a), "a"(b))
#define PINV(x) asm volatile("" : "+v"(x))
#define PINS(x) asm volatile("" : "+s"(x))
#define PREF(dst, voff, srd, soff, IMM) asm volatile("buffer_load_dwordx4 %0, %1, %2, %3 offen offset:" #IMM " sc1" : "+v"(dst) : "v"(voff), "s"(srd), "s"(soff) : "memory")
#define st_by(v, rs, voff, soff) st_sc1(v, rs, voff, soff)
DEV u4 make_srd(const void* p, unsigned bytes) {
    const unsigned long long a = (unsigned long long)p;
    u4 r = {(unsigned)a, (unsigned)(a >> 32) & 0xffffu, bytes, 0x00020000u};
    return r;
}
#define SB() __builtin_amdgcn_sched_barrier(0)

DEV unsigned ld_flag(rsrc_t rw, unsigned off) { return __builtin_amdgcn_raw_buffer_load_b32(rw, 0, off, 16); }
DEV void add_flag(char* ws, unsigned off) { (void)__hip_atomic_fetch_add((unsigned*)(ws + off), 1u, __ATOMIC_RELAXED, __HIP_MEMORY_SCOPE_AGENT); }
DEV bool wait_flag(rsrc_t rw, unsigned off, unsigned target, int& cap) {
    int sp = 0;
    for (;;) {
        asm volatile("" ::: "memory");
        const unsigned v = __builtin_amdgcn_readfirstlane(ld_flag(rw, off));
        if (v >= target) return true;
        if (++sp > cap) { cap = 0; return false; }
        __builtin_amdgcn_s_sleep(8);
    }
}
DEV void drain() { asm volatile("s_waitcnt vmcnt(0)" ::: "memory"); }
constexpr int POL_SW = 8;
constexpr unsigned POL_WORD = 1024 + 512;
DEV unsigned hop_base(int layer, int pol, int s) {
    const int page = layer * 2 + (s >> 5);
    const unsigned bit = (layer == 0 && s < POL_SW) ? 0u : ((unsigned)pol >> page) & 1u;
    return (unsigned)page * (2u << 20) + (unsigned)(s & 31) * HOP_STEP + bit * 8192u;
}
DEV void probe_body(const RecArgs& a) {
    const int tid = threadIdx.x, lane = tid & 63, w = __builtin_amdgcn_readfirstlane(tid >> 6);
    const rsrc_t rs = __builtin_amdgcn_make_buffer_rsrc(a.ws + OFF_HOP, 0, 8u << 20, 0x00020000);
    const rsrc_t rw = __builtin_amdgcn_make_buffer_rsrc(a.ws, 0, (unsigned)WS_END, 0x00020000);
    int cap = SPIN_CAP;
    if (w == 0) (void)wait_flag(rw, flag_off(0, FKM, 0), 32, cap);
    __builtin_amdgcn_s_barrier();
    const unsigned base = (unsigned)w * (2u << 20) + 32 * HOP_STEP + 16384;
    int votes = 0; unsigned sum0 = 0, sum1 = 0;
    for (int i = 0; i < 7; ++i) {
        unsigned d[2];
#pragma unroll
        for (int p = 0; p < 2; ++p) {
            const unsigned off = lane < 8 ? p * 8192u + i * 1152u + lane * 16u : 0x7FFFFF00u;
            st_sc1(u4{SENT, SENT, SENT, SENT}, rs, off, base);
            unsigned long long t0, t1;
            asm volatile("s_waitcnt vmcnt(0)\n\ts_memtime %0\n\ts_waitcnt lgkmcnt(0)" : "=s"(t0) :: "memory");
            u4 v = ld_sc1(rs, off, base);
            asm volatile("s_waitcnt vmcnt(0)\n\ts_memtime %0\n\ts_waitcnt lgkmcnt(0)" : "=s"(t1) : "v"(v) : "memory");
            d[p] = (unsigned)(t1 - t0);
        }
        votes += d[1] < d[0] ? 1 : -1; sum0 += d[0]; sum1 += d[1];
    }
    LAS int* l = (LAS int*)(size_t)0;
    if (lane == 0) { l[w] = votes; l[4 + w] = (int)(sum0 / 7); l[8 + w] = (int)(sum1 / 7); }
    lds_barrier();
    if (tid == 0) {
        const unsigned pol = (l[0] > 0 ? 1u : 0u) | (l[1] > 0 ? 2u : 0u) | (l[2] > 0 ? 4u : 0u) | (l[3] > 0 ? 8u : 0u);
        __hip_atomic_store((unsigned*)(a.ws + OFF_CTL + POL_WORD), 0x100u | pol, __ATOMIC_RELAXED, __HIP_MEMORY_SCOPE_AGENT);
    }
    lds_barrier();
}
DEV int read_pol(const RecArgs& a, rsrc_t rw, int& cap) {
    {
    int sp = 0;
    for (;;) {
        asm volatile("" ::: "memory");
        const unsigned v = __builtin_amdgcn_readfirstlane(ld_flag(rw, (unsigned)OFF_CTL + POL_WORD));
        if (v & 0x100u) return (int)(v & 15u);
        if (++sp > cap) { cap = 0; return 0; }
        __builtin_amdgcn_s_sleep(4);
    }
    }
}

#define P_DELTA 3
#define P_UP 2
#define P_PAT 2
constexpr int DELTA = P_DELTA, D_UP = P_UP, D_PATIENCE = P_PAT, D_MAX = 160;
constexpr unsigned CAN_M = 7168, CAN_S = 7424;
DEV void adapt(int& D, int& early, int passes, unsigned probe) {
    if (passes > 1) { D = D + D_UP < D_MAX ? D + D_UP : D_MAX; early = 0; }
    else if (__builtin_amdgcn_readfirstlane(probe) != SENT) { if (++early >= D_PATIENCE) { early = 0; D = D > DELTA + 1 ? D - 1 : D; } }
    else early = 0;
}
DEV unsigned ld_probe(rsrc_t rs, unsigned off, unsigned base) { return __builtin_amdgcn_raw_buffer_load_b32(rs, off, base, 16); }
DEV bool gather_m(rsrc_t rs, unsigned base, unsigned off0, unsigned off1, u4& f0, u4& f1, int& cap, unsigned& npass, int& sp) {
    sp = 0;
    for (;;) {
        COUNT_PASS(npass);
        asm volatile("" ::: "memory");
        f0 = ld_sc1(rs, off0, base);
        f1 = ld_sc1(rs, off1, base);
        const unsigned m = umax3(umax3(f0.x, f0.y, f0.z), umax3(f0.w, f1.x, f1.y), umax3(f1.z, f1.w, 0u));
        ++sp;
        if (!__any(m == SENT)) return true;
        if (sp > cap) { cap = 0; return false; }
    }
}

#define SPARSE_GEMM(acc, wt, f0, f1, la, idx, EXTRA_READS, EPI0) do {                                 \
        u4 m2 = lds_ld(la[0]), m3 = lds_ld(la[1]), m4 = lds_ld(la[2]), m5 = lds_ld(la[3]), m6 = lds_ld(la[4]); \
        EXTRA_READS;                                                                                 \
        SB();                                                                                        \
        SMF(acc[0], f0, wt[0][0], idx); SMF(acc[1], f0, wt[1][0], idx); SMF(acc[2], f0, wt[2][0], idx); SMF(acc[3], f0, wt[3][0], idx); \
        SMF(acc[0], f1, wt[0][1], idx); SMF(acc[1], f1, wt[1][1], idx); SMF(acc[2], f1, wt[2][1], idx); SMF(acc[3], f1, wt[3][1], idx); \
        SMF(acc[0], m2, wt[0][2], idx); SMF(acc[1], m2, wt[1][2], idx); SMF(acc[0], m3, wt[0][3], idx); SMF(acc[1], m3, wt[1][3], idx); \
        SMF(acc[0], m4, wt[0][4], idx); SMF(acc[1], m4, wt[1][4], idx); SMF(acc[0], m5, wt[0][5], idx); SMF(acc[1], m5, wt[1][5], idx); \
        SMF(acc[0], m6, wt[0][6], idx); SMF(acc[1], m6, wt[1][6], idx);                              \
        SMF(acc[2], m2, wt[2][2], idx); SMF(acc[3], m2, wt[3][2], idx); SMF(acc[2], m3, wt[2][3], idx); SMF(acc[3], m3, wt[3][3], idx); \
        SMF(acc[2], m4, wt[2][4], idx); SMF(acc[3], m4, wt[3][4], idx);                              \
        asm volatile("s_nop 1" : "+v"(acc[0]), "+v"(acc[1]));        \
        SB();                                                                                        \
        SMF(acc[2], m5, wt[2][5], idx); SMF(acc[3], m5, wt[3][5], idx); SMF(acc[2], m6, wt[2][6], idx); SMF(acc[3], m6, wt[3][6], idx); \
        EPI0;                                                                                        \
        SB();                                                                                        \
        asm volatile("s_nop 7" : "+v"(acc[2]), "+v"(acc[3]));                                        \
    } while (0)

DEV void halfsum4(const f4& a, const f4& b, float* x) {
#pragma unroll
    for (int r = 0; r < 4; ++r) {
        auto sw = __builtin_amdgcn_permlane32_swap(__float_as_uint(a[r]), __float_as_uint(b[r]), false, false);
        x[r] = __uint_as_float(sw[0]) + __uint_as_float(sw[1]);
    }
}

template <int P>
DEV void score_body(const RecArgs& a, int h) {
    char* ws = a.ws;
    const int tid = threadIdx.x, lane = tid & 63, w = __builtin_amdgcn_readfirstlane(tid >> 6);
    const int l15 = lane & 15, kg = lane >> 4;
    const rsrc_t rs = __builtin_amdgcn_make_buffer_rsrc(ws + OFF_HOP, 0, 8u << 20, 0x00020000);
    const rsrc_t rw = __builtin_amdgcn_make_buffer_rsrc(ws, 0, (unsigned)WS_END, 0x00020000);
    constexpr unsigned LM = 0, LK = 7168;
    const int idx = (lane & 8) ? 0xEEEE : 0x4444;
    const bool segwave = (P == 0) && (w == 0);
    const int t0 = 2 * w, t1 = (2 * w + 1) % 7;
    const unsigned off0 = t0 * 1024 + lane * 16, off1 = t1 * 1024 + lane * 16;
    unsigned la[5];
#pragma unroll
    for (int j = 0; j < 5; ++j) la[j] = LM + ((2 * w + 2 + j) % 7) * 1024 + lane * 16;
    unsigned* tmo = (unsigned*)(ws + OFF_CTL);
    int cap = SPIN_CAP; unsigned npass = 0; unsigned long long tstart = 0, cstart = 0;
    constexpr int NK2 = P ? 8 : 6, TP0 = P ? 6 : 0;
    const unsigned mdense = LM + (kg * 8 + (lane & 7)) * 16;
    const unsigned traddr = LK + ((8 * kg + (l15 >> 2)) * 16 + (l15 & 3) * 8);
    const unsigned kmoff = (unsigned)OFF_KM + (h * 14 + TP0) * 512 + (kg * 8 + (lane & 7)) * 16;
    const bool lo8 = !(lane & 8);
    const u4 srdw = make_srd(ws, (unsigned)WS_END);

    if (segwave) {
        u4 ks[28];
        const unsigned ksoff = (unsigned)OFF_KS + h * 28672 + lane * 16;
        if (h == 0) TL(0);
        int pol = a.layer ? read_pol(a, rw, cap) : 0;
        bool allok = wait_flag(rw, flag_off(a.layer, F12K, 0), 56, cap);
        if (h == 0) TL(1);
#pragma unroll
        for (int i = 0; i < 28; ++i) ks[i] = ld_sc1(rw, ksoff + (i & 3) * 1024, (i >> 2) * 4096);
        const int i8 = lane & 7;
        const unsigned rec = (unsigned)h * 1024u + (lo8 ? (i8 * 4 + kg) * 16 : 512u + (i8 * 2 + kg) * 16);
        const bool dost = lo8 || kg < 2;
        const unsigned so = dost ? 16384u + (rec & 8191u) + ((rec >> 13) << 14) : (h == 15 && lane == 63) ? CAN_S : 0x7FFFFF00u;
        ST_DECL;
        unsigned fl = 0;
        auto step = [&](int s, auto lastc) {
            constexpr bool LAST = decltype(lastc)::value;
            if (s == POL_SW - 1 && a.layer == 0) pol = read_pol(a, rw, cap);
            const unsigned base = hop_base(a.layer, pol, s);
            u4 f0, f1;
            STAMP(0);
            if (s > 0) __builtin_amdgcn_s_barrier();
            STAMP(1);
            int np;
            bool ok = gather_m(rs, base, off0, off1, f0, f1, cap, npass, np);
            STAMP(2);
            lds_st(LM + off0, f0); lds_st(LM + off1, f1);
            lds_barrier();
            STAMP(3);
            u4 md[14];
#pragma unroll
            for (int i = 0; i < 14; ++i) md[i] = lds_ld(mdense + i * 512);
            SB();
            f4 d0, d1;
            MF0(d0, ks[0], md[0]); MF0(d1, ks[14], md[0]);
#pragma unroll
            for (int i = 1; i < 14; ++i) { MF(d0, ks[i], md[i]); MF(d1, ks[14 + i], md[i]); }
            asm volatile("s_nop 7" : "+v"(d0), "+v"(d1));
            float mx = fmaxf(fmaxf(fmaxf(d0[0], d0[1]), fmaxf(d0[2], d0[3])), fmaxf(fmaxf(d1[0], d1[1]), fmaxf(d1[2], d1[3])));
            mx = swap16_partner_max(mx); mx = swap32_partner_max(mx);
            PINV(mx);
            STAMP(4);
            lds_barrier();
            STAMP(5);
            bf4 klo[NK2], khi[NK2];
#pragma unroll
            for (int i = 0; i < NK2; ++i) {
                klo[i] = __builtin_amdgcn_ds_read_tr16_b64_v4bf16((LAS bf4*)(size_t)(traddr + i * 512));
                khi[i] = __builtin_amdgcn_ds_read_tr16_b64_v4bf16((LAS bf4*)(size_t)(traddr + i * 512 + 64));
                SB();
            }
            float e0 = ex2(d0[0] - mx), e1 = ex2(d0[1] - mx), e2 = ex2(d0[2] - mx), e3 = ex2(d0[3] - mx);
            float e4 = ex2(d1[0] - mx), e5 = ex2(d1[1] - mx), e6 = ex2(d1[2] - mx), e7 = ex2(d1[3] - mx);
            SB();
            u4 kf[NK2];
#pragma unroll
            for (int i = 0; i < NK2; ++i) { bf8 t = {klo[i][0], klo[i][1], klo[i][2], klo[i][3], khi[i][0], khi[i][1], khi[i][2], khi[i][3]}; kf[i] = __builtin_bit_cast(u4, t); }
            f4 d2;
            MF0(d2, kf[0], md[TP0]);
#pragma unroll
            for (int i = 1; i < NK2; ++i) MF(d2, kf[i], md[TP0 + i]);
            float sm = ((e0 + e1) + (e2 + e3)) + ((e4 + e5) + (e6 + e7));
            sm = swap16_partner_add(sm); sm = swap32_partner_add(sm);
            u4 ev = {nosent(pk(e0, e1)), nosent(pk(e2, e3)), nosent(pk(e4, e5)), nosent(pk(e6, e7))};
            PINV(ev); PINV(sm);
            asm volatile("s_nop 7" : "+v"(d2));
            u4 cv = {nosent(pk(d2[0], d2[1])), nosent(pk(d2[2], d2[3])), nosent(__float_as_uint(mx)), nosent(__float_as_uint(sm))};
            u4 v = lo8 ? ev : cv;
            if (!LAST) st_sc1(v, rs, so, base);
            STAMP(6);
            SB();
            if (s > 0 && lane == 0) add_flag(ws, flag_off(a.layer, FKM, s - 1));
            if (lo8) {
#pragma unroll
                for (int i = 0; i < NK2; ++i) st_by(kf[i], rw, kmoff + i * 512, s * (16 * 14 * 512));
            }
            if (LAST) { drain(); if (lane == 0) add_flag(ws, flag_off(a.layer, FKM, s)); }
            if (!LAST) {
                if (__builtin_amdgcn_readfirstlane(fl) < 56) ok = wait_flag(rw, flag_off(a.layer, F12K, s + 1), 56, cap) && ok;
                fl = ld_flag(rw, flag_off(a.layer, F12K, s + 2 < NSEG ? s + 2 : s + 1));
#pragma unroll
                for (int i = 0; i < 7; ++i) {
                    const unsigned so2 = (s + 1) * 458752 + i * 4096;
                    PREF(ks[4 * i], ksoff, srdw, so2, 0); PREF(ks[4 * i + 1], ksoff, srdw, so2, 1024);
                    PREF(ks[4 * i + 2], ksoff, srdw, so2, 2048); PREF(ks[4 * i + 3], ksoff, srdw, so2, 3072);
                }
            }
            allok = allok && ok;
            if (h == 0 && s == 0) TL(2);
            if (h == 0 && s == 8) TL(10);
            if (h == 0 && s == 24) TL(11);
            if (h == 0 && s == 40) TL(12);
            if (h == 0 && LAST) TL(3);
            ST_STEP(0, h == 0);
        };
        for (int s = 0; s < NSEG - 1; ++s) step(s, BoolC<false>());
        step(NSEG - 1, BoolC<true>());
        if (!allok && lane == 0) atomicAdd(tmo, 1u);
    } else {
        const int T0 = P ? 12 + 4 * w : 4 * (w - 1);
        bf16v wt[4][7];
#pragma unroll
        for (int i = 0; i < 4; ++i)
#pragma unroll
            for (int j = 0; j < 7; ++j) {
                const char* p = ws + OFF_WAF + (((size_t)h * 28 + T0 + i) * 7 + (2 * w + j) % 7) * 2048 + lane * 16;
                wt[i][j] = join16(*(const u4*)p, *(const u4*)(p + 1024));
                asm volatile("" : "+a"(wt[i][j]));
            }
        const int aloc0 = 16 * (T0 - (P ? 12 : 0));
        const unsigned kwaddr = LK + (aloc0 + 16 * (lane >> 5) + l15) * 16 + ((lane >> 4) & 1) * 8;
        const unsigned rec = (unsigned)h * 1024u + 768u + ((lane & 7) * 2 + kg) * 16;
        const unsigned so = (lo8 && kg < 2) ? 16384u + (rec & 8191u) + ((rec >> 13) << 14) : 0x7FFFFF00u;
        const bool s2wave = (P == 1 && w == 0);
        int D = a.dA, early = 0;
        int pol = a.layer ? read_pol(a, rw, cap) : 0;
        bool allok = true; ST_DECL;
        auto step = [&](int s, auto lastc) {
            constexpr bool LAST = decltype(lastc)::value;
            if (s == POL_SW - 1 && a.layer == 0) pol = read_pol(a, rw, cap);
            const unsigned base = hop_base(a.layer, pol, s);
            u4 f0, f1;
            f4 acc[4];
#pragma unroll
            for (int i = 0; i < 4; ++i) { acc[i] = f4{0, 0, 0, 0}; PINV(acc[i]); }
            STAMP(0);
            unsigned probe = SENT;
            if (s > 0) {
                if (w == 3) { delay16(D - DELTA); probe = ld_probe(rs, CAN_M, base); delay16(DELTA); }
                __builtin_amdgcn_s_barrier();
            }
            STAMP(1);
            int np;
            const bool ok = gather_m(rs, base, off0, off1, f0, f1, cap, npass, np);
            STAMP(2);
            lds_st(LM + off0, f0); lds_st(LM + off1, f1);
            lds_barrier();
            STAMP(3);
            u4 md[NK2];
            auto epi = [&](int pr) {
                float x[4];
                halfsum4(acc[2 * pr], acc[2 * pr + 1], x);
                u2 v = {pk(x[0], x[1]), pk(x[2], x[3])};
                *(LAS u2*)(size_t)(kwaddr + pr * 512) = v;
            };
            SPARSE_GEMM(acc, wt, f0, f1, la, idx,
                        if (s2wave) { _Pragma("unroll") for (int i = 0; i < NK2; ++i) md[i] = lds_ld(mdense + (TP0 + i) * 512); },
                        epi(0));
            STAMP(4);
            epi(1);
            STAMP(5);
            lds_barrier();
            STAMP(6);
            if (s2wave) {
                bf4 klo[NK2], khi[NK2];
#pragma unroll
                for (int i = 0; i < NK2; ++i) {
                    klo[i] = __builtin_amdgcn_ds_read_tr16_b64_v4bf16((LAS bf4*)(size_t)(traddr + i * 512));
                    khi[i] = __builtin_amdgcn_ds_read_tr16_b64_v4bf16((LAS bf4*)(size_t)(traddr + i * 512 + 64));
                    SB();
                }
                u4 kf[NK2];
#pragma unroll
                for (int i = 0; i < NK2; ++i) { bf8 t = {klo[i][0], klo[i][1], klo[i][2], klo[i][3], khi[i][0], khi[i][1], khi[i][2], khi[i][3]}; kf[i] = __builtin_bit_cast(u4, t); }
                f4 d2;
                MF0(d2, kf[0], md[0]);
#pragma unroll
                for (int i = 1; i < NK2; ++i) MF(d2, kf[i], md[i]);
                asm volatile("s_nop 7" : "+v"(d2));
                u4 v = {nosent(__float_as_uint(d2[0])), nosent(__float_as_uint(d2[1])), nosent(__float_as_uint(d2[2])), nosent(__float_as_uint(d2[3]))};
                if (!LAST) st_sc1(v, rs, so, base);
                STAMP(7);
                SB();
                if (s > 0 && lane == 0) add_flag(ws, flag_off(a.layer, FKM, s - 1));
                if (lo8) {
#pragma unroll
                    for (int i = 0; i < NK2; ++i) st_by(kf[i], rw, kmoff + i * 512, s * (16 * 14 * 512));
                }
                if (LAST) { drain(); if (lane == 0) add_flag(ws, flag_off(a.layer, FKM, s)); }
            }
            allok = allok && ok;
            if (w == 3 && s > 0) adapt(D, early, np, probe);
            ST_STEP(1 + P * 2 + w, h == 0 && w < 2);
        };
        for (int s = 0; s < NSEG - 1; ++s) step(s, BoolC<false>());
        step(NSEG - 1, BoolC<true>());
        if (!allok && lane == 0) atomicAdd(tmo, 1u);
    }
}

DEV void value_body(const RecArgs& a, int c) {
    char* ws = a.ws;
    const int tid = threadIdx.x, lane = tid & 63, w = __builtin_amdgcn_readfirstlane(tid >> 6);
    const int kg = lane >> 4;
    const rsrc_t rs = __builtin_amdgcn_make_buffer_rsrc(ws + OFF_HOP, 0, 8u << 20, 0x00020000);
    const rsrc_t rw = __builtin_amdgcn_make_buffer_rsrc(ws, 0, (unsigned)WS_END, 0x00020000);
    constexpr unsigned LM = 0, LR = 7168;
    const int idx = (lane & 8) ? 0xEEEE : 0x4444;
    const int t0 = 2 * w, t1 = (2 * w + 1) % 7;
    const unsigned off0 = t0 * 1024 + lane * 16, off1 = t1 * 1024 + lane * 16;
    unsigned la[5];
#pragma unroll
    for (int j = 0; j < 5; ++j) la[j] = LM + ((2 * w + 2 + j) % 7) * 1024 + lane * 16;
    unsigned* tmo = (unsigned*)(ws + OFF_CTL);
    int cap = SPIN_CAP; unsigned npass = 0, npass2 = 0;
    bf16v wt[4][7];
#pragma unroll
    for (int i = 0; i < 4; ++i)
#pragma unroll
        for (int j = 0; j < 7; ++j) {
            const char* p = ws + OFF_WBF + (((size_t)c * 16 + 4 * w + i) * 7 + (2 * w + j) % 7) * 2048 + lane * 16;
            wt[i][j] = join16(*(const u4*)p, *(const u4*)(p + 1024));
            asm volatile("" : "+a"(wt[i][j]));
        }
    const int hi8 = (lane >> 3) & 1, i8 = lane & 7;
    unsigned oE0, oE1, oP0, oP1;
    { unsigned rec = (4 * w + 2 * hi8) * 1024u + (i8 * 4 + kg) * 16; oE0 = 16384u + (rec & 8191u) + ((rec >> 13) << 14); oE1 = oE0 + 1024; }
    { const unsigned rec = (4 * w + 2 * hi8 + (kg >> 1)) * 1024u + 512u + (i8 * 2 + (kg & 1)) * 16;
      oP0 = 16384u + (rec & 8191u) + ((rec >> 13) << 14); oP1 = oP0 + 256; }
    const unsigned vsoff = (unsigned)OFF_VS + (c * 16 + 4 * w) * 1024 + lane * 16;
    const unsigned vmoff = (unsigned)OFF_VM + (c * 4 + w) * 1024 + lane * 16;
    const unsigned lrw = LR + w * 512 + (kg * 8 + i8) * 16;
    const int ep = lane & 3, ii = (lane >> 2) & 7, hf = lane >> 5;
    const unsigned lrr = LR + (((2 * hf + (ep >> 1)) * 8 + ii) * 4 + 2 * (ep & 1)) * 4;
    const unsigned mo = ep == 0 ? (unsigned)c * 256u + hf * 128u + ii * 16u : (c == 27 && lane == 63) ? CAN_M : 0x7FFFFF00u;
    const u4 srdw = make_srd(ws, (unsigned)WS_END);
    u4 vs[4];
    int pol = a.layer ? read_pol(a, rw, cap) : 0;
    bool allok = wait_flag(rw, flag_off(a.layer, F12V, 0), 56, cap);
#pragma unroll
    for (int i = 0; i < 4; ++i) vs[i] = ld_sc1(rw, vsoff + i * 1024, 0);
    unsigned fl = 0;
    int D = a.dB2, early = 0;
    ST_DECL;
    auto step = [&](int s, auto lastc) {
        constexpr bool LAST = decltype(lastc)::value;
        if (s == POL_SW - 1 && a.layer == 0) pol = read_pol(a, rw, cap);
        const unsigned base = hop_base(a.layer, pol, s);
        unsigned nb = hop_base(a.layer, pol, s + 1); PINS(nb);
        u4 f0, f1;
        f4 acc[4];
#pragma unroll
        for (int i = 0; i < 4; ++i) { acc[i] = f4{0, 0, 0, 0}; PINV(acc[i]); }
        STAMP(0);
        if (s > 0) delay16(a.dB1);
        int np;
        bool ok = gather_m(rs, base, off0, off1, f0, f1, cap, npass, np);
        if constexpr (LAST) asm volatile("" :: "v"(vs[0]), "v"(vs[1]), "v"(vs[2]), "v"(vs[3]));
        STAMP(1);
        lds_st(LM + off0, f0); lds_st(LM + off1, f1);
        lds_barrier();
        STAMP(2);
        u4 vm;
        {
            float x[4], y[4];
            SPARSE_GEMM(acc, wt, f0, f1, la, idx, , halfsum4(acc[0], acc[1], x));
            halfsum4(acc[2], acc[3], y);
            vm = u4{pk(x[0], x[1]), pk(x[2], x[3]), pk(y[0], y[1]), pk(y[2], y[3])};
        }
        st_by(vm, rw, vmoff, s * (28 * 4 * 1024));
        if constexpr (LAST) {
            drain(); lds_barrier();
            if (w == 1 && lane == 0) add_flag(ws, flag_off(a.layer, FVM, s));
            allok = allok && ok; return;
        }
        STAMP(3);
        delay16(D - DELTA);
        const unsigned probe = ld_probe(rs, CAN_S, base);
        delay16(DELTA);
        u4 e0, e1, p0, p1;
        int sp = 0;
        {
            for (;;) {
                COUNT_PASS(npass2);
                asm volatile("" ::: "memory");
                p0 = ld_sc1(rs, oP0, base);
                p1 = ld_sc1(rs, oP1, base);
                e0 = ld_sc1(rs, oE0, base);
                e1 = ld_sc1(rs, oE1, base);
                const unsigned m = umax3(umax3(umax3(p0.x, p0.y, p0.z), umax3(p0.w, p1.x, p1.y), umax3(p1.z, p1.w, e0.x)),
                                         umax3(umax3(e0.y, e0.z, e0.w), e1.x, e1.y), umax3(e1.z, e1.w, 0u));
                ++sp;
                if (!__any(m == SENT)) break;
                if (sp > cap) { cap = 0; ok = false; break; }
            }
        }
        STAMP(4);
        f4 da0, db0, da1, db1;
        MF0(da0, vs[0], e0); MF0(db0, vs[2], e0); MF0(da1, vs[1], e1); MF0(db1, vs[3], e1);
        SB();
        const float s0 = bflo(p0.x) + __uint_as_float(p1.x), s1 = bfhi(p0.x) + __uint_as_float(p1.y);
        const float s2 = bflo(p0.y) + __uint_as_float(p1.z), s3 = bfhi(p0.y) + __uint_as_float(p1.w);
        const float mloc = __uint_as_float(p0.z), sloc = __uint_as_float(p0.w);
        float M = fmaxf(fmaxf(fmaxf(s0, s1), fmaxf(s2, s3)), mloc);
        M = swap16_partner_max(M);
        const float x0 = ex2(s0 - M), x1 = ex2(s1 - M), x2 = ex2(s2 - M), x3 = ex2(s3 - M), f = ex2(mloc - M);
        float l = (x0 + x1) + (x2 + x3);
        l = swap16_partner_add(l);
        l = fmaf(f, sloc, l);
        const float inv = __builtin_amdgcn_rcpf(l);
        const unsigned q0 = pk(x0 * inv, x1 * inv), q1 = pk(x2 * inv, x3 * inv);
        u4 pb;
        pb.x = hi8 ? 0u : q0; pb.y = hi8 ? 0u : q1;
        pb.z = __builtin_amdgcn_update_dpp(0u, q0, 0x108  , 0xf, 0xf, true);
        pb.w = __builtin_amdgcn_update_dpp(0u, q1, 0x108, 0xf, 0xf, true);
        f4 o;
        MF0N(o, vm, pb);
        const float scv = f * inv;
        auto sw = __builtin_amdgcn_permlane32_swap(__float_as_uint(scv), __float_as_uint(scv), false, false);
        const float sc0 = __uint_as_float(sw[0]), sc1 = __uint_as_float(sw[1]);
        asm volatile("" : "+v"(da0), "+v"(db0), "+v"(da1), "+v"(db1));
        float t[4];
#pragma unroll
        for (int r = 0; r < 4; ++r) t[r] = sc0 * (hi8 ? db0[r] : da0[r]) + sc1 * (hi8 ? db1[r] : da1[r]);
        asm volatile("s_nop 7" : "+v"(o), "+v"(t[0]), "+v"(t[1]), "+v"(t[2]), "+v"(t[3]));
#pragma unroll
        for (int r = 0; r < 4; ++r) {
            float v = t[r] + o[r];
            v += __uint_as_float(__builtin_amdgcn_update_dpp(0u, __float_as_uint(v), 0x128  , 0xf, 0xf, false));
            o[r] = v;
        }
        STAMP(5);
        *(LAS f4*)(size_t)lrw = o;
        lds_barrier();
        STAMP(6);
        if (w == 1 && lane == 0) add_flag(ws, flag_off(a.layer, FVM, s));
        if (w == 0) {
            typedef float f2 __attribute__((ext_vector_type(2)));
            f2 r0 = *(LAS const f2*)(size_t)lrr, r1 = *(LAS const f2*)(size_t)(lrr + 512), r2 = *(LAS const f2*)(size_t)(lrr + 1024), r3 = *(LAS const f2*)(size_t)(lrr + 1536);
            const unsigned d = nosent(pk((r0[0] + r1[0]) + (r2[0] + r3[0]), (r0[1] + r1[1]) + (r2[1] + r3[1])));
            u4 v;
            v.x = d;
            v.y = __builtin_amdgcn_update_dpp(0u, d, 0x55  , 0xf, 0xf, false);
            v.z = __builtin_amdgcn_update_dpp(0u, d, 0xAA, 0xf, 0xf, false);
            v.w = __builtin_amdgcn_update_dpp(0u, d, 0xFF, 0xf, 0xf, false);
            st_sc1(v, rs, mo, nb);
        }
        STAMP(7);
        SB();
        if (__builtin_amdgcn_readfirstlane(fl) < 56) ok = wait_flag(rw, flag_off(a.layer, F12V, s + 1), 56, cap) && ok;
        fl = ld_flag(rw, flag_off(a.layer, F12V, s + 2 < NSEG ? s + 2 : s + 1));
        adapt(D, early, sp, probe);
        { const unsigned so2 = (s + 1) * (28 * 16 * 1024);
          PREF(vs[0], vsoff, srdw, so2, 0); PREF(vs[1], vsoff, srdw, so2, 1024); PREF(vs[2], vsoff, srdw, so2, 2048); PREF(vs[3], vsoff, srdw, so2, 3072); }
        allok = allok && ok;
        ST_STEP(5 + w, c == 0 && w < 2);
    };
    for (int s = 0; s < NSEG - 1; ++s) step(s, BoolC<false>());
    step(NSEG - 1, BoolC<true>());
    if (!allok && lane == 0) atomicAdd(tmo, 1u);
    if (c == 0 && w < 2) ST_OUT(5 + w);
}

#define GLDS16(src, dst) glds16((src), (dst))
template <bool isK>
DEV void w12_body(const RecArgs& a, int blk) {
    char* ws = a.ws;
    const int tid = threadIdx.x, lane = tid & 63, w = __builtin_amdgcn_readfirstlane(tid >> 6);
    const int wu = blk * 4 + w;
    const int l15 = lane & 15, kg = lane >> 4;
    const rsrc_t rw = __builtin_amdgcn_make_buffer_rsrc(ws, 0, (unsigned)WS_END, 0x00020000);
    u4 wt[2][14];
    unsigned so0, so1, sstride;
    if (isK) {
        const int h = wu / 14, tp = wu % 14;
#pragma unroll
        for (int u = 0; u < 2; ++u) {
            const int aa = 32 * tp + 8 * (l15 >> 2) + 4 * u + (l15 & 3);
#pragma unroll
            for (int ks = 0; ks < 14; ++ks)
                wt[u][ks] = *(const u4*)(ws + OFF_WAF + ((((size_t)h * 28 + (aa >> 4)) * 14 + ks) * 64 + kg * 16 + (aa & 15)) * 16);
        }
        so0 = (unsigned)OFF_KS + (h * 28 + tp) * 1024 + lane * 16; so1 = so0 + 14336; sstride = 458752;
    } else {
        const int idx = wu - 224, h = idx & 15, c0 = 2 * (idx >> 4);
#pragma unroll
        for (int u = 0; u < 2; ++u)
#pragma unroll
            for (int ks = 0; ks < 14; ++ks)
                wt[u][ks] = *(const u4*)(ws + OFF_WBF + ((((size_t)(c0 + u) * 16 + h) * 14 + ks) * 64 + lane) * 16);
        so0 = (unsigned)OFF_VS + (c0 * 16 + h) * 1024 + lane * 16; so1 = so0 + 16384; sstride = 458752;
    }
    const char* xsrc = ws + OFF_XP + w * 7168 + lane * 16;
    auto stage = [&](int s) {
#pragma unroll
        for (int i = 0; i < 7; ++i) GLDS16(xsrc + (size_t)s * 28672 + i * 1024, (s & 1) * 28672 + w * 7168 + i * 1024);
    };
    const unsigned fo = flag_off(a.layer, isK ? F12K : F12V, 0);
    if (blk == 0) TL(4);
    stage(0);
    for (int s = 0; s < NSEG; ++s) {
        if (s == 0) asm volatile("s_waitcnt vmcnt(0)" ::: "memory"); else asm volatile("s_waitcnt vmcnt(2)" ::: "memory");
        __builtin_amdgcn_s_barrier();
        asm volatile("" ::: "memory");
        if (s >= 3 && tid == 0) add_flag(ws, fo + (s - 2) * 128);
        if (s + 1 < NSEG) stage(s + 1);
        const unsigned lb = (s & 1) * 28672 + lane * 16;
        f4 c00, c01, c10, c11;
        u4 x0[14], x1[14];
#pragma unroll
        for (int ks = 0; ks < 14; ++ks) { x0[ks] = lds_ld(lb + ks * 1024); x1[ks] = lds_ld(lb + 14336 + ks * 1024); }
        SB();
        if (isK) { MF0(c00, wt[0][0], x0[0]); MF0(c10, wt[1][0], x0[0]); MF0(c01, wt[0][0], x1[0]); MF0(c11, wt[1][0], x1[0]); }
        else { MF0(c00, x0[0], wt[0][0]); MF0(c01, x1[0], wt[0][0]); MF0(c10, x0[0], wt[1][0]); MF0(c11, x1[0], wt[1][0]); }
#pragma unroll
        for (int ks = 1; ks < 14; ++ks) {
            if (isK) { MF(c00, wt[0][ks], x0[ks]); MF(c10, wt[1][ks], x0[ks]); MF(c01, wt[0][ks], x1[ks]); MF(c11, wt[1][ks], x1[ks]); }
            else { MF(c00, x0[ks], wt[0][ks]); MF(c01, x1[ks], wt[0][ks]); MF(c10, x0[ks], wt[1][ks]); MF(c11, x1[ks], wt[1][ks]); }
        }
        asm volatile("s_nop 7" : "+v"(c00), "+v"(c01), "+v"(c10), "+v"(c11));
        u4 v0, v1;
        if (isK) {
            v0 = u4{pk(c00[0], c00[1]), pk(c00[2], c00[3]), pk(c10[0], c10[1]), pk(c10[2], c10[3])};
            v1 = u4{pk(c01[0], c01[1]), pk(c01[2], c01[3]), pk(c11[0], c11[1]), pk(c11[2], c11[3])};
        } else {
            v0 = u4{pk(c00[0], c00[1]), pk(c00[2], c00[3]), pk(c01[0], c01[1]), pk(c01[2], c01[3])};
            v1 = u4{pk(c10[0], c10[1]), pk(c10[2], c10[3]), pk(c11[0], c11[1]), pk(c11[2], c11[3])};
        }
        st_sc1(v0, rw, so0, s * sstride); st_sc1(v1, rw, so1, s * sstride);
        asm volatile("s_waitcnt lgkmcnt(0)" ::: "memory");
        if (s == 0) {
            drain(); __builtin_amdgcn_s_barrier();
            if (tid == 0) add_flag(ws, fo);
            if (blk == 0) TL(5);
        }
    }
    drain(); __builtin_amdgcn_s_barrier();
    if (tid == 0) { add_flag(ws, fo + (NSEG - 2) * 128); add_flag(ws, fo + (NSEG - 1) * 128); }
    TLMAX(6);
}

DEV void w3a_body(const RecArgs& a, int j) {
    char* ws = a.ws;
    const int tid = threadIdx.x, lane = tid & 63, w = __builtin_amdgcn_readfirstlane(tid >> 6);
    const rsrc_t rw = __builtin_amdgcn_make_buffer_rsrc(ws, 0, (unsigned)WS_END, 0x00020000);
    const int l15 = lane & 15, kg = lane >> 4, h = (j & 3) * 4 + w;
    int cap = SPIN_CAP; bool ok = true;
    for (int s = j >> 2; s < NSEG; s += 4) {
        if (w == 0) ok = wait_flag(rw, flag_off(a.layer, F12K, s), 56, cap) && ok;
        __builtin_amdgcn_s_barrier();
        asm volatile("" ::: "memory");
        const unsigned kb = (unsigned)OFF_KS + (s * 16 + h) * 28672 + lane * 16;
        const unsigned mb = (unsigned)OFF_KM + (s * 16 + h) * 7168 + (kg * 8 + (lane & 7)) * 16;
        const char* xb = ws + OFF_XN + (size_t)s * 28672 + lane * 16;
        u4 x0[14], x1[14];
#pragma unroll
        for (int t = 0; t < 14; ++t) { x0[t] = *(const u4*)(xb + t * 1024); x1[t] = *(const u4*)(xb + 14336 + t * 1024); }
        f4 d00, d01, d10, d11, d20, d21;
        {
            u4 k0[14], k1[14];
#pragma unroll
            for (int t = 0; t < 14; ++t) { k0[t] = ld_sc1(rw, kb, t * 1024); k1[t] = ld_sc1(rw, kb + 14336, t * 1024); }
            MF0B(d00, k0[0], x0[0]); MF0B(d01, k0[0], x1[0]); MF0B(d10, k1[0], x0[0]); MF0B(d11, k1[0], x1[0]);
#pragma unroll
            for (int t = 1; t < 14; ++t) { MFB(d00, k0[t], x0[t]); MFB(d01, k0[t], x1[t]); MFB(d10, k1[t], x0[t]); MFB(d11, k1[t], x1[t]); }
        }
        if (w == 0) ok = wait_flag(rw, flag_off(a.layer, FKM, s), 32, cap) && ok;
        __builtin_amdgcn_s_barrier();
        asm volatile("" ::: "memory");
        {
            u4 km[14];
#pragma unroll
            for (int t = 0; t < 14; ++t) km[t] = ld_sc1(rw, mb, t * 512);
            MF0B(d20, km[0], x0[0]); MF0B(d21, km[0], x1[0]);
#pragma unroll
            for (int t = 1; t < 14; ++t) { MFB(d20, km[t], x0[t]); MFB(d21, km[t], x1[t]); }
        }
        asm volatile("s_nop 7" : "+v"(d00), "+v"(d01), "+v"(d10), "+v"(d11), "+v"(d20), "+v"(d21));
        const float NEG = -3.0e38f;
#pragma unroll
        for (int qt = 0; qt < 2; ++qt) {
            const f4 a0 = qt ? d01 : d00, a1 = qt ? d11 : d10, a2 = qt ? d21 : d20;
            const int iq = 16 * qt + l15;
            float v[12];
#pragma unroll
            for (int r = 0; r < 4; ++r) {
                v[r] = (4 * kg + r <= iq) ? a0[r] : NEG;
                v[4 + r] = (16 + 4 * kg + r <= iq) ? a1[r] : NEG;
                v[8 + r] = (kg < 2) ? a2[r] : NEG;
            }
            float mx = v[0];
#pragma unroll
            for (int r = 1; r < 12; ++r) mx = fmaxf(mx, v[r]);
            mx = swap16_partner_max(mx); mx = swap32_partner_max(mx);
            float sm = 0;
#pragma unroll
            for (int r = 0; r < 12; ++r) { v[r] = ex2(v[r] - mx); sm += v[r]; }
            sm = swap16_partner_add(sm); sm = swap32_partner_add(sm);
            const float inv = 1.0f / sm;
            u4 ps = {pk(v[0] * inv, v[1] * inv), pk(v[2] * inv, v[3] * inv), pk(v[4] * inv, v[5] * inv), pk(v[6] * inv, v[7] * inv)};
            st_sc1(ps, rw, (unsigned)OFF_PS + ((s * 16 + h) * 2 + qt) * 1024 + lane * 16, 0);
            if (kg < 2) {
                u2 pm = {pk(v[8] * inv, v[9] * inv), pk(v[10] * inv, v[11] * inv)};
                const int hl = h & 3;
                __builtin_amdgcn_raw_buffer_store_b64(pm, rw, (unsigned)OFF_PM + ((s * 4 + (h >> 2)) * 2 + qt) * 1024 + ((2 * (hl & 1) + kg) * 16 + l15) * 16 + 8 * (hl >> 1), 0, 16);
            }
        }
        drain(); __builtin_amdgcn_s_barrier();
        if (tid == 0) add_flag(ws, flag_off(a.layer, FPS, s));
    }
    TLMAX(7);
    if (!ok && lane == 0) atomicAdd((unsigned*)(ws + OFF_CTL), 1u);
}

DEV void w3b_body(const RecArgs& a, int j) {
    char* ws = a.ws;
    const int tid = threadIdx.x, lane = tid & 63, w = __builtin_amdgcn_readfirstlane(tid >> 6);
    const rsrc_t rw = __builtin_amdgcn_make_buffer_rsrc(ws, 0, (unsigned)WS_END, 0x00020000);
    const int l15 = lane & 15, kg = lane >> 4, qt = w & 1, c0 = (j % 7) * 4 + (w >> 1) * 2;
    float* x32 = (float*)(ws + OFF_X32);
    int cap = SPIN_CAP; bool ok = true;
    for (int s = j / 7; s < NSEG; s += 9) {
        if (w == 0) ok = wait_flag(rw, flag_off(a.layer, F12V, s), 56, cap) && ok;
        __builtin_amdgcn_s_barrier();
        asm volatile("" ::: "memory");
        const unsigned vb = (unsigned)OFF_VS + (s * 28 + c0) * 16384 + lane * 16;
        const unsigned mb = (unsigned)OFF_VM + (s * 28 + c0) * 4096 + lane * 16;
        u4 av[2][16];
#pragma unroll
        for (int u = 0; u < 2; ++u)
#pragma unroll
            for (int h = 0; h < 16; ++h) {
                av[u][h] = ld_sc1(rw, vb + u * 16384, h * 1024);
            }
        float* xp = x32 + (size_t)(32 * s + 16 * qt + l15) * DM + 16 * c0 + 4 * kg;
        f4 xv0 = *(f4*)xp, xv1 = *(f4*)(xp + 16);
        if (w == 0) {
            ok = wait_flag(rw, flag_off(a.layer, FVM, s), 28, cap) && ok;
            ok = wait_flag(rw, flag_off(a.layer, FPS, s), 4, cap) && ok;
        }
        __builtin_amdgcn_s_barrier();
        asm volatile("" ::: "memory");
        u4 ps[16], pm[4], am[2][4];
#pragma unroll
        for (int h = 0; h < 16; ++h) {
            ps[h] = ld_sc1(rw, (unsigned)OFF_PS + ((s * 16 + h) * 2 + qt) * 1024 + lane * 16, 0);
        }
#pragma unroll
        for (int g = 0; g < 4; ++g) {
            pm[g] = ld_sc1(rw, (unsigned)OFF_PM + ((s * 4 + g) * 2 + qt) * 1024 + lane * 16, 0);
            am[0][g] = ld_sc1(rw, mb, g * 1024); am[1][g] = ld_sc1(rw, mb + 4096, g * 1024);
        }
        f4 d0, d1;
        MF0A(d0, av[0][0], ps[0]); MF0A(d1, av[1][0], ps[0]);
#pragma unroll
        for (int h = 1; h < 16; ++h) { MFA(d0, av[0][h], ps[h]); MFA(d1, av[1][h], ps[h]); }
#pragma unroll
        for (int g = 0; g < 4; ++g) { MF(d0, am[0][g], pm[g]); MF(d1, am[1][g], pm[g]); }
        asm volatile("s_nop 7" : "+v"(d0), "+v"(d1));
        *(f4*)xp = xv0 + d0; *(f4*)(xp + 16) = xv1 + d1;
    }
    TLMAX(8);
    if (!ok && lane == 0) atomicAdd((unsigned*)(ws + OFF_CTL), 1u);
}

__global__ void __launch_bounds__(256, 1) k_rec(RecArgs a) {
    const int b = blockIdx.x, x = b & 7, r = b >> 3;
    if (b == 0 && threadIdx.x < 64) TL(9);
    if (x == 0) { if (r & 1) score_body<1>(a, r >> 1); else score_body<0>(a, r >> 1); }
    else if (x == 1) { if (r < 28) value_body(a, r); else if (r == 28 && a.layer == 0) probe_body(a); }
    else {
        const int wr = r * 6 + (x - 2);
        if (wr < 112) { if (a.xs & 1) { if (wr < 56) w12_body<true>(a, wr); else w12_body<false>(a, wr); } }
        else if (wr < 128) { if (a.xs & 2) w3a_body(a, wr - 112); }
        else if (wr < 128 + 63) { if (a.xs & 4) w3b_body(a, wr - 128); }
    }
}

__global__ void __launch_bounds__(256, 1) k_b1(char* __restrict__ ws) {
    __shared__ __attribute__((aligned(16))) float xr[16 * XLD];
    __shared__ __attribute__((aligned(16))) u16 xb[16 * 456];
    const int tile = blockIdx.x >> 1, nh = blockIdx.x & 1, tid = threadIdx.x, lane = tid & 63, w = tid >> 6;
    const float* x32 = (const float*)(ws + OFF_X32);
    KST(0, 0);
    f4 xin[7];
#pragma unroll
    for (int k = 0; k < 7; ++k) { const int i = tid + 256 * k, r = i / 112, c = (i % 112) * 4; xin[k] = *(const f4*)(x32 + (size_t)(tile * 16 + r) * DM + c); }
    u4 wf[4][14];
#pragma unroll
    for (int ti = 0; ti < 4; ++ti)
#pragma unroll
        for (int ks = 0; ks < 14; ++ks) wf[ti][ks] = *(const u4*)(ws + OFF_WQF + ((size_t)(nh * 16 + w * 4 + ti) * 14 + ks) * 1024 + lane * 16);
#pragma unroll
    for (int k = 0; k < 7; ++k) { const int i = tid + 256 * k, r = i / 112, c = (i % 112) * 4; *(f4*)(xr + r * XLD + c) = xin[k]; }
    __syncthreads();
    KST(0, 1);
    ln_rows_to_frags(xr, xb, tile, nullptr, tid);
    KST(0, 2);
    const int l15 = lane & 15, kg = lane >> 4;
    bf8 xf[14];
#pragma unroll
    for (int ks = 0; ks < 14; ++ks) xf[ks] = asbf8(*(const u4*)(xb + l15 * 456 + 32 * ks + 8 * kg));
    const int tok = tile * 16 + l15;
#pragma unroll
    for (int ti = 0; ti < 4; ++ti) {
        const int T = nh * 16 + w * 4 + ti;
        f4 d = {0, 0, 0, 0};
        if (T < 30) {
#pragma unroll
            for (int ks = 0; ks < 14; ++ks) d = mfma16(asbf8(wf[ti][ks]), xf[ks], d);
            if (T < 28) {
                const float sc = LOG2E * 0.18898223650461362f;
                const int n = 16 * T + 4 * kg, h = n / 28, dd = n % 28;
                u16* q = (u16*)(ws + OFF_BQ) + ((size_t)h * SEQ + tok) * 32 + dd;
                u2 v = {pk(d[0] * sc, d[1] * sc), pk(d[2] * sc, d[3] * sc)};
                *(u2*)q = v;
                if (dd == 24) *(u2*)(q + 4) = u2{0, 0};
            } else {
                const int dd = 16 * (T - 28) + 4 * kg;
                char* kp = ws + OFF_BK + (size_t)(tok >> 5) * 2048 + (dd >> 4) * 1024 + (((dd >> 3) & 1) * 32 + (tok & 31)) * 16 + (dd & 7) * 2;
                *(u2*)kp = u2{pk(d[0], d[1]), pk(d[2], d[3])};
            }
        } else {
#pragma unroll
            for (int ks = 0; ks < 14; ++ks) d = mfma16(xf[ks], asbf8(wf[ti][ks]), d);
            const int dd = 16 * (T - 30) + l15;
            if (dd == 28) d = f4{1.f, 1.f, 1.f, 1.f};
            char* vp = ws + OFF_BV + (size_t)(tile >> 1) * 2048 + (tile & 1) * 1024 + ((kg & 1) * 32 + dd) * 16 + 8 * (kg >> 1);
            *(u2*)vp = u2{pk(d[0], d[1]), pk(d[2], d[3])};
        }
    }
    KST(0, 3);
}

constexpr float THR = 10.0f;
#define MF32C(d, a, b, c) asm volatile("v_mfma_f32_32x32x16_bf16 %0, %1, %2, %3" : "=&v"(d) : "v"(a), "v"(b), "v"(c))
#define MF32(d, a, b) asm volatile("v_mfma_f32_32x32x16_bf16 %0, %1, %2, %0" : "+v"(d) : "v"(a), "v"(b))
#define MF32N(d, a, b) asm volatile("s_nop 1\n\tv_mfma_f32_32x32x16_bf16 %0, %1, %2, %0" : "+v"(d) : "v"(a), "v"(b))
DEV float max16(const f16v& s) {
    float m;
    asm("v_max3_f32 %0, %1, %2, %3\n\tv_max3_f32 %0, %0, %4, %5\n\tv_max3_f32 %0, %0, %6, %7\n\tv_max3_f32 %0, %0, %8, %9\n\t"
        "v_max3_f32 %0, %0, %10, %11\n\tv_max3_f32 %0, %0, %12, %13\n\tv_max3_f32 %0, %0, %14, %15\n\tv_max_f32 %0, %0, %16"
        : "=&v"(m) : "v"(s[0]), "v"(s[1]), "v"(s[2]), "v"(s[3]), "v"(s[4]), "v"(s[5]), "v"(s[6]), "v"(s[7]), "v"(s[8]), "v"(s[9]),
          "v"(s[10]), "v"(s[11]), "v"(s[12]), "v"(s[13]), "v"(s[14]), "v"(s[15]));
    return m;
}
#define B2_TILE(sc, sn, kb, vb) do {                                                                            \
        const u4 kn0 = lds_ld(kb), kn1 = lds_ld((kb) + 1024), v0 = lds_ld(vb), v1 = lds_ld((vb) + 1024);         \
        float mx = max16(sc);                                                                                   \
        if (__builtin_expect(__any(mx > THR), 0)) {     \
            mx = swap32_partner_max(mx);                                                                        \
            const float dlt = fmaxf(mx, 0.f), al = ex2(-dlt);                                                   \
            m += dlt;                                                                                           \
            asm volatile("s_nop 7\n\ts_nop 3" : "+v"(o));                                                       \
            _Pragma("unroll") for (int r = 0; r < 16; ++r) { o[r] *= al; sc[r] -= dlt; negm[r] = -m; }           \
            asm volatile("s_nop 1" : "+v"(o), "+v"(negm), "+v"(sc));                                            \
        }                                                                                                       \
        MF32C(sn, kn0, q0, negm);                                                                               \
        u4 p0 = u4{pk(ex2(sc[0]), ex2(sc[1])), pk(ex2(sc[2]), ex2(sc[3])), pk(ex2(sc[4]), ex2(sc[5])), pk(ex2(sc[6]), ex2(sc[7]))}; \
        PINV(p0);                                                                                               \
        MF32(sn, kn1, q1);                                                                                      \
        MF32N(o, v0, p0);                                                                                       \
        u4 p1 = u4{pk(ex2(sc[8]), ex2(sc[9])), pk(ex2(sc[10]), ex2(sc[11])), pk(ex2(sc[12]), ex2(sc[13])), pk(ex2(sc[14]), ex2(sc[15]))}; \
        MF32N(o, v1, p1);                                                                                       \
    } while (0)
__global__ void __launch_bounds__(1024, 4) k_b2(char* __restrict__ ws) {
    const int tid = threadIdx.x, lane = tid & 63, w = __builtin_amdgcn_readfirstlane(tid >> 6);
    const int half = blockIdx.x & 1, qg = blockIdx.x >> 1, sub = w >> 3;
    KST(1, 0);
    const int qt = qg * 8 + (w & 7), h = qt >> 6, tt = qt & 63;
    const int l31 = lane & 31, hh = lane >> 5;
    const u16* qp = (const u16*)(ws + OFF_BQ) + ((size_t)h * SEQ + tt * 32 + l31) * 32 + 8 * hh;
    u4 q0, q1;
    asm volatile("global_load_dwordx4 %0, %2, off\n\tglobal_load_dwordx4 %1, %2, off offset:32" : "=&v"(q0), "=&v"(q1) : "v"(qp) : "memory");
    {
#pragma unroll
        for (int g = 0; g < 4; ++g)
#pragma unroll
            for (int e = 0; e < 2; ++e) {
                const int p = 2 * w + e, tile = 16 * (p >> 4) + 4 * g + ((p >> 2) & 3), part = p & 3;
                const unsigned off = (part >> 1) * 65536 + tile * 2048 + (part & 1) * 1024;
                const char* src = ws + ((part >> 1) ? OFF_BV : OFF_BK) + (size_t)half * 65536 + tile * 2048 + (part & 1) * 1024 + lane * 16;
                glds16(src, off);
            }
    }
    asm volatile("s_waitcnt vmcnt(6)" : "+v"(q0), "+v"(q1) :: "memory");
    __builtin_amdgcn_s_barrier();
    asm volatile("" ::: "memory");
    KST(1, 1);
    f16v o, negm, sA, sB;
#pragma unroll
    for (int r = 0; r < 16; ++r) { o[r] = 0.f; negm[r] = 0.f; }
    float m;
    const unsigned lb = lane * 16 + sub * 32768;
    {
        const u4 k0 = lds_ld(lb), k1 = lds_ld(lb + 1024);
        PINV(negm); PINV(o);
        asm volatile("s_nop 1" ::: "memory");
        MF32C(sA, k0, q0, negm); MF32(sA, k1, q1);
        asm volatile("s_nop 7\n\ts_nop 3" : "+v"(sA));
        m = swap32_partner_max(max16(sA));
#pragma unroll
        for (int r = 0; r < 16; ++r) { negm[r] = -m; sA[r] -= m; }
        asm volatile("s_nop 1" : "+v"(negm), "+v"(sA));
    }
#pragma unroll 1
    for (int g = 0; g < 4; ++g) {
        if (g == 0) asm volatile("s_waitcnt vmcnt(4)" ::: "memory"); else if (g == 1) asm volatile("s_waitcnt vmcnt(2)" ::: "memory"); else asm volatile("s_waitcnt vmcnt(0)" ::: "memory");
        __builtin_amdgcn_s_barrier();
        asm volatile("" ::: "memory");
#pragma unroll 1
        for (int i = 0; i < 2; ++i) {
            const unsigned kt = 4 * g + 2 * i;
            B2_TILE(sA, sB, lb + (kt + 1) * 2048, 65536 + lb + kt * 2048);
            B2_TILE(sB, sA, lb + ((kt + 2) & 15) * 2048, 65536 + lb + (kt + 1) * 2048);
        }
    }
    KST(1, 2);
    asm volatile("s_nop 7\n\ts_nop 3" : "+v"(o));
    __builtin_amdgcn_s_barrier();
    const unsigned mo = (w & 7) * 5120 + lane * 16;
    if (sub == 1) {
#pragma unroll
        for (int g = 0; g < 4; ++g) *(LAS f4*)(size_t)(mo + g * 1024) = f4{o[4 * g], o[4 * g + 1], o[4 * g + 2], o[4 * g + 3]};
        *(LAS float*)(size_t)((w & 7) * 5120 + 4096 + lane * 4) = m;
    }
    lds_barrier();
    if (sub == 0) {
        const float m1 = *(LAS const float*)(size_t)((w & 7) * 5120 + 4096 + lane * 4);
        const float M = fmaxf(m, m1), a0 = ex2(m - M), a1 = ex2(m1 - M);
#pragma unroll
        for (int g = 0; g < 4; ++g) {
            const f4 t = *(LAS const f4*)(size_t)(mo + g * 1024);
#pragma unroll
            for (int r = 0; r < 4; ++r) o[4 * g + r] = o[4 * g + r] * a0 + t[r] * a1;
        }
        const float lsum_own = o[12];
        auto sw = __builtin_amdgcn_permlane32_swap(__float_as_uint(lsum_own), __float_as_uint(lsum_own), false, false);
        const float l = __uint_as_float(sw[1]);
        const float inv = 1.0f / l;
        const int tok = tt * 32 + l31;
        u16* op = (u16*)(ws + OFF_BO) + ((size_t)half * SEQ + tok) * DM + h * 28 + 4 * hh;
#pragma unroll
        for (int g = 0; g < 4; ++g) {
            if (g < 3 || hh == 0) *(u2*)(op + 8 * g) = u2{pk(o[4 * g] * inv, o[4 * g + 1] * inv), pk(o[4 * g + 2] * inv, o[4 * g + 3] * inv)};
        }
        if (hh == 0) { typedef float f2 __attribute__((ext_vector_type(2))); *(f2*)(ws + OFF_BS + (((size_t)half * 16 + h) * SEQ + tok) * 8) = f2{M, l}; }
    }
    KST(1, 3);
}

__global__ void __launch_bounds__(256, 1) k_b3(char* __restrict__ ws, u16* __restrict__ out, int final_) {
    __shared__ __attribute__((aligned(16))) float xr[16 * XLD];
    __shared__ __attribute__((aligned(16))) u16 xb[16 * 456];
    const int tile = blockIdx.x, tid = threadIdx.x, lane = tid & 63, w = tid >> 6;
    typedef float f2 __attribute__((ext_vector_type(2)));
    const int l15 = lane & 15, kg = lane >> 4;
    float* x32 = (float*)(ws + OFF_X32);
    const int tok = tile * 16 + l15;
    KST(2 + final_, 0);
    u4 oa[4], ob[4]; f2 sa[4][2], sb[4][2];
#pragma unroll
    for (int k = 0; k < 4; ++k) {
        const int i = tid + 256 * k < 896 ? tid + 256 * k : 895, r = i / 56, c8 = (i % 56) * 8, tk = tile * 16 + r;
        oa[k] = *(const u4*)((const u16*)(ws + OFF_BO) + (size_t)tk * DM + c8);
        ob[k] = *(const u4*)((const u16*)(ws + OFF_BO) + ((size_t)SEQ + tk) * DM + c8);
#pragma unroll
        for (int e = 0; e < 2; ++e) {
            const int h = (c8 + 4 * e) / 28;
            sa[k][e] = *(const f2*)(ws + OFF_BS + ((size_t)h * SEQ + tk) * 8); sb[k][e] = *(const f2*)(ws + OFF_BS + ((size_t)(16 + h) * SEQ + tk) * 8);
        }
    }
    u4 wf[4][14];
#pragma unroll
    for (int ti = 0; ti < 4; ++ti)
#pragma unroll
        for (int ks = 0; ks < 14; ++ks) wf[ti][ks] = *(const u4*)(ws + OFF_WOF + ((size_t)(w * 7 + ti) * 14 + ks) * 1024 + lane * 16);
    f4 xv[7];
#pragma unroll
    for (int ti = 0; ti < 7; ++ti) xv[ti] = *(const f4*)(x32 + (size_t)tok * DM + 16 * (w * 7 + ti) + 4 * kg);
#pragma unroll
    for (int k = 0; k < 4; ++k) {
        const int i = tid + 256 * k;
        if (i < 896) {
            const int r = i / 56, c8 = (i % 56) * 8;
            float wa[2], wb[2];
#pragma unroll
            for (int e = 0; e < 2; ++e) {
                const float M = fmaxf(sa[k][e][0], sb[k][e][0]);
                const float la = sa[k][e][1] * ex2(sa[k][e][0] - M), lb = sb[k][e][1] * ex2(sb[k][e][0] - M);
                const float inv = 1.0f / (la + lb);
                wa[e] = la * inv; wb[e] = lb * inv;
            }
            const u4 a = oa[k], b = ob[k];
            u4 o;
            o.x = pk(wa[0] * bflo(a.x) + wb[0] * bflo(b.x), wa[0] * bfhi(a.x) + wb[0] * bfhi(b.x));
            o.y = pk(wa[0] * bflo(a.y) + wb[0] * bflo(b.y), wa[0] * bfhi(a.y) + wb[0] * bfhi(b.y));
            o.z = pk(wa[1] * bflo(a.z) + wb[1] * bflo(b.z), wa[1] * bfhi(a.z) + wb[1] * bfhi(b.z));
            o.w = pk(wa[1] * bflo(a.w) + wb[1] * bflo(b.w), wa[1] * bfhi(a.w) + wb[1] * bfhi(b.w));
            *(u4*)(xb + r * 456 + c8) = o;
        }
    }
    __syncthreads();
    KST(2 + final_, 1);
    bf8 xf[14];
#pragma unroll
    for (int ks = 0; ks < 14; ++ks) xf[ks] = asbf8(*(const u4*)(xb + l15 * 456 + 32 * ks + 8 * kg));
#pragma unroll
    for (int ti = 0; ti < 7; ++ti) {
        const int T = w * 7 + ti;
        f4 d = {0, 0, 0, 0};
#pragma unroll
        for (int ks = 0; ks < 14; ++ks) d = mfma16(asbf8(wf[ti & 3][ks]), xf[ks], d);
        if (ti < 3) {
#pragma unroll
            for (int ks = 0; ks < 14; ++ks) wf[ti][ks] = *(const u4*)(ws + OFF_WOF + ((size_t)(T + 4) * 14 + ks) * 1024 + lane * 16);
        }
        const f4 v = xv[ti] + d;
        if (final_) *(u2*)(out + (size_t)tok * DM + 16 * T + 4 * kg) = u2{pk(v[0], v[1]), pk(v[2], v[3])};
        else { *(f4*)(x32 + (size_t)tok * DM + 16 * T + 4 * kg) = v; *(f4*)(xr + l15 * XLD + 16 * T + 4 * kg) = v; }
    }
    KST(2 + final_, 2);
    if (!final_) {
        __syncthreads();
        ln_rows_to_frags(xr, xb, tile, ws, tid);
    }
    KST(2 + final_, 3);
}

#define P_POL 1
#define P_DA 64
#define P_DB1 8
#define P_DB2 20
extern "C" void kernel_launch(void* const* d_in, const int* in_sizes, int n_in, void* d_out, int out_size, void* d_ws, size_t ws_size,
                              hipStream_t stream) {
    const u16* x = (const u16*)d_in[0];
    const u16* mem0 = (const u16*)d_in[1];
    const u16 *Wq = (const u16*)d_in[2], *Wk = (const u16*)d_in[3], *Wv = (const u16*)d_in[4], *Wo = (const u16*)d_in[5];
    const u16 *wq = (const u16*)d_in[8], *wk = (const u16*)d_in[9], *wv = (const u16*)d_in[10], *wo = (const u16*)d_in[11];
    char* ws = (char*)(((uintptr_t)d_ws + (2 * MB - 1)) & ~(uintptr_t)(2 * MB - 1));
    PrepArgs pa;
    pa.x = x; pa.mem0 = mem0; pa.Wq = Wq; pa.Wk = Wk; pa.Wv = Wv; pa.Wo = Wo; pa.wq = wq; pa.wk = wk; pa.wv = wv; pa.wo = wo; pa.ws = ws;
    k_prep<<<512, 256, 65536, stream>>>(pa);
    hipFuncSetAttribute((const void*)k_rec, hipFuncAttributeMaxDynamicSharedMemorySize, 96 * 1024);
    hipFuncSetAttribute((const void*)k_b2, hipFuncAttributeMaxDynamicSharedMemorySize, 128 * 1024);
    for (int layer = 0; layer < 2; ++layer) {
        RecArgs ra;
        ra.ws = ws; ra.layer = layer; ra.pol = P_POL; ra.xs = 7; ra.xv = 1; ra.dA = P_DA; ra.dB1 = P_DB1; ra.dB2 = P_DB2; ra.pad = 0;
        k_rec<<<256, 256, 96 * 1024, stream>>>(ra);
        k_b1<<<256, 256, 0, stream>>>(ws);
        k_b2<<<256, 1024, 128 * 1024, stream>>>(ws);
        k_b3<<<128, 256, 0, stream>>>(ws, (u16*)d_out, layer);
    }
}
```

```cpp
#include <hip/hip_runtime.h>
#include <hip/hip_bf16.h>
#include <stdint.h>

typedef unsigned short u16;
typedef __bf16 bf8 __attribute__((ext_vector_type(8)));
typedef __bf16 bf16v __attribute__((ext_vector_type(16)));
typedef __bf16 bf4 __attribute__((ext_vector_type(4)));
typedef __bf16 bf2 __attribute__((ext_vector_type(2)));
typedef float f4 __attribute__((ext_vector_type(4)));
typedef float f16v __attribute__((ext_vector_type(16)));
typedef unsigned u4 __attribute__((ext_vector_type(4)));
typedef unsigned u2 __attribute__((ext_vector_type(2)));
#define LAS __attribute__((address_space(3)))
#define DEV __device__ __forceinline__

constexpr int SEQ = 2048, DM = 448, NH = 16, DH = 256, NSEG = 64;
constexpr float LOG2E = 1.4426950408889634f;

constexpr size_t MB = 1u << 20;
constexpr size_t OFF_HOP = 0;
constexpr size_t OFF_WAF = 8 * MB;
constexpr size_t OFF_WBF = OFF_WAF + 6422528;
constexpr size_t OFF_XN = OFF_WBF + 6422528;
constexpr size_t OFF_XP = OFF_XN + 1835008;
constexpr size_t OFF_KS = OFF_XP + 1835008;
constexpr size_t OFF_VS = OFF_KS + 29360128;
constexpr size_t OFF_KM = OFF_VS + 29360128;
constexpr size_t OFF_VM = OFF_KM + 7340032;
constexpr size_t OFF_PS = OFF_VM + 7340032;
constexpr size_t OFF_PM = OFF_PS + 2097152;
constexpr size_t OFF_X32 = OFF_PM + 524288;
constexpr size_t OFF_WQF = OFF_X32 + 3670016;
constexpr size_t OFF_WOF = OFF_WQF + 458752;
constexpr size_t OFF_BQ = OFF_WOF + 401408;
constexpr size_t OFF_BK = OFF_BQ + 2097152;
constexpr size_t OFF_BV = OFF_BK + 131072;
constexpr size_t OFF_BO = OFF_BV + 131072;
constexpr size_t OFF_BS = OFF_BO + 3670016;
constexpr size_t OFF_BN = OFF_BS + 524288;
constexpr size_t OFF_CTL = OFF_BN + 4096;
constexpr size_t CTL_BYTES = 262144;
constexpr size_t WS_END = OFF_CTL + CTL_BYTES;
enum { F12K = 0, F12V = 1, FKM = 2, FVM = 3, FPS = 4, FXR = 5, FXO = 6, FXQ = 7, NFLAG = 8 };
constexpr unsigned FLAG0 = 65536;
__host__ __device__ inline unsigned flag_off(int layer, int type, int s) { return (unsigned)OFF_CTL + FLAG0 + (unsigned)((layer * NFLAG + type) * 64 + s) * 128u; }

constexpr unsigned SENT = 0xFFFFFFFFu;
constexpr unsigned HOP_STEP = 49152;

DEV unsigned pk(float a, float b) { bf2 v = {(__bf16)a, (__bf16)b}; return __builtin_bit_cast(unsigned, v); }
DEV float bflo(unsigned w) { return __uint_as_float(w << 16); }
DEV float bfhi(unsigned w) { return __uint_as_float(w & 0xffff0000u); }
DEV bf8 asbf8(u4 v) { return __builtin_bit_cast(bf8, v); }
DEV f4 mfma16(bf8 a, bf8 b, f4 c) { return __builtin_amdgcn_mfma_f32_16x16x32_bf16(a, b, c, 0, 0, 0); }
DEV bf16v join16(u4 lo, u4 hi) {
    typedef unsigned u8v __attribute__((ext_vector_type(8)));
    u8v t = {lo.x, lo.y, lo.z, lo.w, hi.x, hi.y, hi.z, hi.w};
    return __builtin_bit_cast(bf16v, t);
}
DEV float swap32_partner_max(float v) {
    auto r = __builtin_amdgcn_permlane32_swap(__float_as_uint(v), __float_as_uint(v), false, false);
    return fmaxf(__uint_as_float(r[0]), __uint_as_float(r[1]));
}
DEV float swap32_partner_add(float v) {
    auto r = __builtin_amdgcn_permlane32_swap(__float_as_uint(v), __float_as_uint(v), false, false);
    return __uint_as_float(r[0]) + __uint_as_float(r[1]);
}
DEV float swap16_partner_max(float v) {
    auto r = __builtin_amdgcn_permlane16_swap(__float_as_uint(v), __float_as_uint(v), false, false);
    return fmaxf(__uint_as_float(r[0]), __uint_as_float(r[1]));
}
DEV float swap16_partner_add(float v) {
    auto r = __builtin_amdgcn_permlane16_swap(__float_as_uint(v), __float_as_uint(v), false, false);
    return __uint_as_float(r[0]) + __uint_as_float(r[1]);
}
DEV float ex2(float x) { return __builtin_amdgcn_exp2f(x); }
DEV void lds_barrier() { asm volatile("s_waitcnt lgkmcnt(0)" ::: "memory"); __builtin_amdgcn_s_barrier(); asm volatile("" ::: "memory"); }
DEV unsigned umax3(unsigned a, unsigned b, unsigned c) { unsigned t = a > b ? a : b; return t > c ? t : c; }
DEV unsigned nosent(unsigned w) { return w < 0xFFFFFFFEu ? w : 0xFFFFFFFEu; }

__host__ __device__ inline int permb(int t, int R, int gB, int j) {
    return 64 * t + 16 * (2 * R + (gB >> 1)) + 8 * ((j >> 1) & 1) + 2 * (2 * (gB & 1) + (j >> 2)) + (j & 1);
}

DEV void glds16(const void* gsrc, unsigned lds_dst) {
    unsigned keep;
    asm volatile("s_mov_b32 %0, m0\n\ts_mov_b32 m0, %2\n\ts_nop 0\n\tglobal_load_lds_dwordx4 %1, off\n\ts_mov_b32 m0, %0" : "=&s"(keep) : "v"(gsrc), "s"(lds_dst) : "memory");
}
DEV void glds16_sc1(const void* gsrc, unsigned lds_dst) {
    unsigned keep;
    asm volatile("s_mov_b32 %0, m0\n\ts_mov_b32 m0, %2\n\ts_nop 0\n\tglobal_load_lds_dwordx4 %1, off sc1\n\ts_mov_b32 m0, %0" : "=&s"(keep) : "v"(gsrc), "s"(lds_dst) : "memory");
}
DEV u4 lds_ld(unsigned addr) { return *(LAS const u4*)(size_t)addr; }
DEV void lds_st(unsigned addr, u4 v) { *(LAS u4*)(size_t)addr = v; }

template <int MAT>
DEV void prep_w_body(int h, int t, int th, const u16* __restrict__ Wa, const u16* __restrict__ Wb, char* __restrict__ ws) {
    const int tid = threadIdx.x, lane = tid & 63, w = __builtin_amdgcn_readfirstlane(tid >> 6);
    const int l15 = lane & 15, kg = lane >> 4;
    const int prow = lane >> 5, ppos = lane & 31;
    auto stage_rows = [&](const u16* base, int row0, int rowstride, unsigned dst) {
#pragma unroll
        for (int p = 0; p < 8; ++p) {
            const int r = 2 * (8 * w + p) + prow;
            glds16(base + ((size_t)(row0 + r) * NH + h) * DH + ((ppos ^ (r & 15)) << 3), dst + (8 * w + p) * 1024);
        }
    };
    const char* slab_src = (const char*)(Wb + ((size_t)h * DH + (lane >> 3)) * DM + (lane & 7) * 8);
    auto stage = [&](int i) {
        if (MAT == 0) stage_rows(Wb, 64 * i, 1, (i & 1) * 32768);
        else {
#pragma unroll
            for (int p = 0; p < 8; ++p) glds16(slab_src + ((size_t)(8 * (8 * w + p)) * DM + 64 * i) * 2, (i & 1) * 32768 + (8 * w + p) * 1024);
        }
    };
    const int i0 = th ? 4 : 0, i1 = th ? 7 : 4;
    stage_rows(Wa, 64 * t, 1, (1 - (i0 & 1)) * 32768);
    stage(i0);
    asm volatile("s_waitcnt vmcnt(8)" ::: "memory");
    __builtin_amdgcn_s_barrier();
    asm volatile("" ::: "memory");
    bf8 av[2][2][8];
#pragma unroll
    for (int R = 0; R < 2; ++R)
#pragma unroll
        for (int u = 0; u < 2; ++u) {
            const int row = permb(0, R, l15 >> 2, 4 * u + (l15 & 3));
#pragma unroll
            for (int ke = 0; ke < 8; ++ke) av[R][u][ke] = asbf8(lds_ld((1 - (i0 & 1)) * 32768 + row * 512 + (((4 * ke + kg) ^ (row & 15)) << 4)));
        }
    asm volatile("s_waitcnt lgkmcnt(0)" ::: "memory");
    const float sc = MAT ? 1.0f : LOG2E / 16.0f;
    const unsigned tra = (8 * kg + (l15 >> 2)) * 128 + (16 * w + 4 * (l15 & 3)) * 2;
#pragma unroll 1
    for (int i = i0; i < i1; ++i) {
        const int T = 4 * i + w;
        bf8 bv[8];
        if (i == i0) asm volatile("s_waitcnt vmcnt(0)" ::: "memory"); else asm volatile("s_waitcnt vmcnt(2)" ::: "memory");
        __builtin_amdgcn_s_barrier();
        asm volatile("" ::: "memory");
        if (i + 1 < i1) stage(i + 1);
        if (MAT == 0) {
            const int r = 16 * w + l15;
#pragma unroll
            for (int ke = 0; ke < 8; ++ke) bv[ke] = asbf8(lds_ld((i & 1) * 32768 + r * 512 + (((4 * ke + kg) ^ (r & 15)) << 4)));
        } else {
#pragma unroll
            for (int ke = 0; ke < 8; ++ke) {
                const unsigned ad = (i & 1) * 32768 + ke * 4096 + tra;
                bf4 lo = __builtin_amdgcn_ds_read_tr16_b64_v4bf16((LAS bf4*)(size_t)ad);
                bf4 hi = __builtin_amdgcn_ds_read_tr16_b64_v4bf16((LAS bf4*)(size_t)(ad + 512));
                bv[ke] = bf8{lo[0], lo[1], lo[2], lo[3], hi[0], hi[1], hi[2], hi[3]};
            }
        }
        __builtin_amdgcn_sched_barrier(0);
        f4 acc[2][2];
#pragma unroll
        for (int R = 0; R < 2; ++R)
#pragma unroll
            for (int u = 0; u < 2; ++u) acc[R][u] = f4{0, 0, 0, 0};
#pragma unroll
        for (int ke = 0; ke < 8; ++ke)
#pragma unroll
            for (int R = 0; R < 2; ++R)
#pragma unroll
                for (int u = 0; u < 2; ++u) acc[R][u] = mfma16(av[R][u][ke], bv[ke], acc[R][u]);
#pragma unroll
        for (int R = 0; R < 2; ++R) {
            u4 v;
            v.x = pk(acc[R][0][0] * sc, acc[R][0][1] * sc); v.y = pk(acc[R][0][2] * sc, acc[R][0][3] * sc);
            v.z = pk(acc[R][1][0] * sc, acc[R][1][1] * sc); v.w = pk(acc[R][1][2] * sc, acc[R][1][3] * sc);
            const size_t fr = MAT ? (((size_t)T * 16 + h) * 7 + t) * 2 + R : (((size_t)h * 28 + T) * 7 + t) * 2 + R;
            *(u4*)(ws + (MAT ? OFF_WBF : OFF_WAF) + fr * 1024 + lane * 16) = v;
        }
        asm volatile("s_waitcnt lgkmcnt(0)" ::: "memory");
    }
}

constexpr unsigned PAGE_USED = 32 * HOP_STEP + 32768;
DEV void hop_init(int layer, int gid, int nthr, const u16* __restrict__ mem0, char* __restrict__ ws) {
    for (int i = gid; i < 2 * (int)(PAGE_USED / 16); i += nthr) {
        const unsigned pg = (unsigned)i / (PAGE_USED / 16), inp = ((unsigned)i % (PAGE_USED / 16)) * 16;
        u4 v = {SENT, SENT, SENT, SENT};
        if (pg == 0 && inp < 16384) {
            const unsigned o = inp & 8191;
            if (o < 7168) {
                const int l = (o >> 4) & 63, tt = o >> 10;
                const int tok = l & 7, col = 64 * tt + 16 * (l >> 4) + 8 * ((l >> 3) & 1);
                v = *(const u4*)(mem0 + tok * DM + col);
                v.x = nosent(v.x); v.y = nosent(v.y); v.z = nosent(v.z); v.w = nosent(v.w);
            }
        }
        *(u4*)(ws + OFF_HOP + (size_t)(layer * 2 + pg) * (2 * MB) + inp) = v;
    }
}
DEV void prep_misc_body(int gid0, int nthr0, const u16* __restrict__ mem0, const u16* __restrict__ wq, const u16* __restrict__ wk,
                        const u16* __restrict__ wv, const u16* __restrict__ wo, char* __restrict__ ws) {
    const int nthr = nthr0 / 2;
    const bool second = gid0 >= nthr;
    const int gid = second ? gid0 - nthr : gid0;
    if (!second) {
    hop_init(0, gid, nthr, mem0, ws);
    for (int i = gid; i < (int)(CTL_BYTES / 16); i += nthr) {
        u4 z = u4{0, 0, 0, 0};
        *(u4*)(ws + OFF_CTL + (size_t)i * 16) = z;
    }
    return;
    }
    for (int i = gid; i < 60 * 14 * 64; i += nthr) {
        const int lane = i & 63, ks = (i >> 6) % 14, T = (i >> 6) / 14;
        const int n = lane & 15, kg = lane >> 4;
        u16 v[8];
#pragma unroll
        for (int j = 0; j < 8; ++j) {
            const int k = 32 * ks + 8 * kg + j;
            u16 x;
            if (T < 28) x = wq[k * 448 + 16 * T + n];
            else if (T < 32) { const int c = 16 * ((T - 28) & 1) + n; const u16* wsrc = T < 30 ? wk : wv; x = c < 28 ? wsrc[k * 28 + c] : (u16)0; }
            else x = wo[k * 448 + 16 * (T - 32) + n];
            v[j] = x;
        }
        u4 o = {v[0] | ((unsigned)v[1] << 16), v[2] | ((unsigned)v[3] << 16), v[4] | ((unsigned)v[5] << 16), v[6] | ((unsigned)v[7] << 16)};
        *(u4*)(ws + OFF_WQF + (size_t)i * 16) = o;
    }
}

constexpr int XLD = DM + 4;
DEV float row16_sum(float v) {
    v += __uint_as_float(__builtin_amdgcn_update_dpp(0u, __float_as_uint(v), 0x128  , 0xf, 0xf, false));
    v += __uint_as_float(__builtin_amdgcn_update_dpp(0u, __float_as_uint(v), 0x124  , 0xf, 0xf, false));
    v += __uint_as_float(__builtin_amdgcn_update_dpp(0u, __float_as_uint(v), 0x122  , 0xf, 0xf, false));
    v += __uint_as_float(__builtin_amdgcn_update_dpp(0u, __float_as_uint(v), 0x121  , 0xf, 0xf, false));
    return v;
}
DEV void ln_rows_to_frags(const float* __restrict__ xr, u16* __restrict__ xb, int tile16, char* __restrict__ ws, int tid) {
    const int lane = tid & 63, w = tid >> 6;
    {
        const int r = w * 4 + (lane >> 4), j = lane & 15;
        f4 v[7];
#pragma unroll
        for (int i = 0; i < 7; ++i) v[i] = *(const f4*)(xr + r * XLD + j * 28 + 4 * i);
        float s = 0;
#pragma unroll
        for (int i = 0; i < 7; ++i) s += (v[i][0] + v[i][1]) + (v[i][2] + v[i][3]);
        const float mean = row16_sum(s) * (1.0f / DM);
        float q = 0;
#pragma unroll
        for (int i = 0; i < 7; ++i) { v[i] -= mean; q += (v[i][0] * v[i][0] + v[i][1] * v[i][1]) + (v[i][2] * v[i][2] + v[i][3] * v[i][3]); }
        const float rstd = 1.0f / sqrtf(row16_sum(q) * (1.0f / DM) + 1e-5f);
#pragma unroll
        for (int i = 0; i < 7; ++i) *(u2*)(xb + r * 456 + j * 28 + 4 * i) = u2{pk(v[i][0] * rstd, v[i][1] * rstd), pk(v[i][2] * rstd, v[i][3] * rstd)};
    }
    __syncthreads();
    if (ws) {
        const int s = tile16 >> 1, kt = tile16 & 1;
        char* xn = ws + OFF_XN + (size_t)(s * 2 + kt) * 14336;
        char* xp = ws + OFF_XP + (size_t)(s * 2 + kt) * 14336;
        const int row = lane & 15, kg = lane >> 4;
        for (int f = w; f < 14; f += 4) {
            *(u4*)(xn + f * 1024 + lane * 16) = *(const u4*)(xb + row * 456 + 32 * f + 8 * kg);
            const int t = f >> 1, R = f & 1;
            const unsigned* base = (const unsigned*)(xb + row * 456 + 64 * t + 16 * (2 * R + (kg >> 1)));
            const int s2 = 2 * (kg & 1);
            u4 v = {base[s2], base[4 + s2], base[s2 + 1], base[5 + s2]};
            *(u4*)(xp + f * 1024 + lane * 16) = v;
        }
    }
}

DEV void ln16(unsigned xr, unsigned xb, int tid) {
    const int lane = tid & 63, w = tid >> 6;
    const int r = w * 4 + (lane >> 4), j = lane & 15;
    f4 v[7];
#pragma unroll
    for (int i = 0; i < 7; ++i) v[i] = *(LAS const f4*)(size_t)(xr + (r * XLD + j * 28 + 4 * i) * 4);
    float s = 0;
#pragma unroll
    for (int i = 0; i < 7; ++i) s += (v[i][0] + v[i][1]) + (v[i][2] + v[i][3]);
    const float mean = row16_sum(s) * (1.0f / DM);
    float q = 0;
#pragma unroll
    for (int i = 0; i < 7; ++i) { v[i] -= mean; q += (v[i][0] * v[i][0] + v[i][1] * v[i][1]) + (v[i][2] * v[i][2] + v[i][3] * v[i][3]); }
    const float rstd = 1.0f / sqrtf(row16_sum(q) * (1.0f / DM) + 1e-5f);
#pragma unroll
    for (int i = 0; i < 7; ++i) *(LAS u2*)(size_t)(xb + (r * 456 + j * 28 + 4 * i) * 2) = u2{pk(v[i][0] * rstd, v[i][1] * rstd), pk(v[i][2] * rstd, v[i][3] * rstd)};
}

DEV void ln0_body(int tile, const u16* __restrict__ x, char* __restrict__ ws, float* xr, u16* xb) {
    const int tid = threadIdx.x;
    for (int i = tid; i < 16 * 56; i += 256) {
        const int r = i / 56, c = (i % 56) * 8;
        u4 v = *(const u4*)(x + (size_t)(tile * 16 + r) * DM + c);
        f4 a = {bflo(v.x), bfhi(v.x), bflo(v.y), bfhi(v.y)}, b = {bflo(v.z), bfhi(v.z), bflo(v.w), bfhi(v.w)};
        *(f4*)(xr + r * XLD + c) = a; *(f4*)(xr + r * XLD + c + 4) = b;
    }
    __syncthreads();
    ln_rows_to_frags(xr, xb, tile, ws, tid);
}
struct PrepArgs { const u16 *x, *mem0, *Wq, *Wk, *Wv, *Wo, *wq, *wk, *wv, *wo; char* ws; };
__global__ void __launch_bounds__(256, 2) k_prep(PrepArgs p) {
    extern __shared__ __attribute__((aligned(16))) char dyn[];
    const int b = blockIdx.x < 64 ? blockIdx.x + 448 : blockIdx.x - 64;
    if (b < 448) {
        const int k = b >> 3, r = k % 28, h = 2 * (b & 7) + r / 14, t = (r % 14) >> 1, th = r & 1;
        if (k < 28) prep_w_body<1>(h, t, th, p.Wv, p.Wo, p.ws); else prep_w_body<0>(h, t, th, p.Wk, p.Wq, p.ws);
    }
    else {
        ln0_body(2 * (b - 448), p.x, p.ws, (float*)dyn, (u16*)(dyn + 16 * XLD * 4));
        __syncthreads();
        ln0_body(2 * (b - 448) + 1, p.x, p.ws, (float*)dyn, (u16*)(dyn + 16 * XLD * 4));
    }
    prep_misc_body(blockIdx.x * 256 + threadIdx.x, 512 * 256, p.mem0, p.wq, p.wk, p.wv, p.wo, p.ws);
}

struct RecArgs {
    char* ws;
    const u16* x0;
    const u16* mem0;
    int layer;
    int dA, dB1, dB2, dC;
    int pad;
};
DEV void delay16(int n) {
    asm volatile("" : "+s"(n));
#pragma unroll 1
    for (int i = 0; i < n; ++i) asm volatile("s_nop 3" ::: "memory");
}
constexpr int SPIN_CAP = 1 << 16;
template <bool B> struct BoolC { static constexpr bool value = B; };
typedef __amdgpu_buffer_rsrc_t rsrc_t;
DEV u4 ld_sc1(rsrc_t rs, unsigned voff, unsigned soff) { return __builtin_amdgcn_raw_buffer_load_b128(rs, voff, soff, 16); }
DEV void st_sc1(u4 v, rsrc_t rs, unsigned voff, unsigned soff) { __builtin_amdgcn_raw_buffer_store_b128(v, rs, voff, soff, 16); }
#define SMF(acc, a, w, idx) asm volatile("v_smfmac_f32_16x16x64_bf16 %0, %1, %2, %3" : "+v"(acc) : "v"(a), "a"(w), "v"(idx))
#define MF0(d, a, b) asm volatile("v_mfma_f32_16x16x32_bf16 %0, %1, %2, 0" : "=&v"(d) : "v"(a), "v"(b))
#define MF0N(d, a, b) asm volatile("s_nop 1\n\tv_mfma_f32_16x16x32_bf16 %0, %1, %2, 0" : "=&v"(d) : "v"(a), "v"(b))
#define MF(d, a, b) asm volatile("v_mfma_f32_16x16x32_bf16 %0, %1, %2, %0" : "+v"(d) : "v"(a), "v"(b))
#define MF0A(d, a, b) asm volatile("v_mfma_f32_16x16x32_bf16 %0, %1, %2, 0" : "=&v"(d) : "a"(a), "v"(b))
#define MFA(d, a, b) asm volatile("v_mfma_f32_16x16x32_bf16 %0, %1, %2, %0" : "+v"(d) : "a"(a), "v"(b))
#define MF0B(d, a, b) asm volatile("v_mfma_f32_16x16x32_bf16 %0, %1, %2, 0" : "=&v"(d) : "v"(a), "a"(b))
#define MFB(d, a, b) asm volatile("v_mfma_f32_16x16x32_bf16 %0, %1, %2, %0" : "+v"(d) : "v"(a), "a"(b))
#define PINV(x) asm volatile("" : "+v"(x))
#define PINS(x) asm volatile("" : "+s"(x))
#define PREF(dst, voff, srd, soff, IMM) asm volatile("buffer_load_dwordx4 %0, %1, %2, %3 offen offset:" #IMM " sc1" : "+v"(dst) : "v"(voff), "s"(srd), "s"(soff) : "memory")
DEV u4 make_srd(const void* p, unsigned bytes) {
    const unsigned long long a = (unsigned long long)p;
    u4 r = {(unsigned)a, (unsigned)(a >> 32) & 0xffffu, bytes, 0x00020000u};
    return r;
}
#define SB() __builtin_amdgcn_sched_barrier(0)

DEV unsigned ld_flag(rsrc_t rw, unsigned off) { return __builtin_amdgcn_raw_buffer_load_b32(rw, 0, off, 16); }
DEV void add_flag(char* ws, unsigned off) { (void)__hip_atomic_fetch_add((unsigned*)(ws + off), 1u, __ATOMIC_RELAXED, __HIP_MEMORY_SCOPE_AGENT); }
DEV bool wait_flag(rsrc_t rw, unsigned off, unsigned target, int& cap, bool tight = false) {
    int sp = 0;
    for (;;) {
        asm volatile("" ::: "memory");
        const unsigned v = __builtin_amdgcn_readfirstlane(ld_flag(rw, off));
        if (v >= target) return true;
        if (++sp > cap) { cap = 0; return false; }
        if (!tight) __builtin_amdgcn_s_sleep(8);
    }
}
DEV void drain() { asm volatile("s_waitcnt vmcnt(0)" ::: "memory"); }
constexpr int POL_SW = 8;
constexpr unsigned POL_WORD = 1024 + 512;
DEV unsigned hop_base(int layer, int pol, int s) {
    const int page = layer * 2 + (s >> 5);
    const unsigned bit = (layer == 0 && s < POL_SW) ? 0u : ((unsigned)pol >> page) & 1u;
    return (unsigned)page * (2u << 20) + (unsigned)(s & 31) * HOP_STEP + bit * 8192u;
}
DEV void probe_body(const RecArgs& a) {
    const int tid = threadIdx.x, lane = tid & 63, w = __builtin_amdgcn_readfirstlane(tid >> 6);
    const rsrc_t rs = __builtin_amdgcn_make_buffer_rsrc(a.ws + OFF_HOP, 0, 8u << 20, 0x00020000);
    const rsrc_t rw = __builtin_amdgcn_make_buffer_rsrc(a.ws, 0, (unsigned)WS_END, 0x00020000);
    int cap = SPIN_CAP;
    if (w == 0) (void)wait_flag(rw, flag_off(0, FKM, 0), 32, cap);
    __builtin_amdgcn_s_barrier();
    const unsigned base = (unsigned)w * (2u << 20) + 32 * HOP_STEP + 16384;
    unsigned best[2] = {0xffffffffu, 0xffffffffu};
    for (int i = 0; i < 7; ++i) {
#pragma unroll
        for (int p = 0; p < 2; ++p) {
            const unsigned off = lane < 8 ? p * 8192u + i * 1152u + lane * 16u : 0x7FFFFF00u;
            st_sc1(u4{SENT, SENT, SENT, SENT}, rs, off, base);
            unsigned long long t0, t1;
            asm volatile("s_waitcnt vmcnt(0)\n\ts_memtime %0\n\ts_waitcnt lgkmcnt(0)" : "=s"(t0) :: "memory");
            u4 v = ld_sc1(rs, off, base);
            asm volatile("s_waitcnt vmcnt(0)\n\ts_memtime %0\n\ts_waitcnt lgkmcnt(0)" : "=s"(t1) : "v"(v) : "memory");
            const unsigned d = (unsigned)(t1 - t0);
            best[p] = d < best[p] ? d : best[p];
        }
    }
    LAS int* l = (LAS int*)(size_t)0;
    if (lane == 0) l[w] = best[1] < best[0] ? 1 : 0;
    lds_barrier();
    if (tid == 0) {
        const unsigned pol = (unsigned)(l[0] | (l[1] << 1) | (l[2] << 2) | (l[3] << 3));
        __hip_atomic_store((unsigned*)(a.ws + OFF_CTL + POL_WORD), 0x100u | pol, __ATOMIC_RELAXED, __HIP_MEMORY_SCOPE_AGENT);
    }
    lds_barrier();
}
DEV int read_pol(const RecArgs& a, rsrc_t rw, int& cap) {
    int sp = 0;
    for (;;) {
        asm volatile("" ::: "memory");
        const unsigned v = __builtin_amdgcn_readfirstlane(ld_flag(rw, (unsigned)OFF_CTL + POL_WORD));
        if (v & 0x100u) return (int)(v & 15u);
        if (++sp > cap) { cap = 0; return 0; }
        __builtin_amdgcn_s_sleep(4);
    }
}

#define A2_UP 1
#define A2_RUN 6
constexpr unsigned CAN_M = 7168;
DEV void wait_canary(rsrc_t rs, unsigned off, unsigned base, int& cap) {
    int sp = 0;
    for (;;) {
        asm volatile("" ::: "memory");
        const unsigned v = __builtin_amdgcn_readfirstlane(__builtin_amdgcn_raw_buffer_load_b32(rs, off, base, 16));
        if (v != SENT) return;
        if (++sp > cap) { cap = 0; return; }
    }
}
DEV void put_canary(rsrc_t rs, bool lane0, unsigned off, unsigned base) { __builtin_amdgcn_raw_buffer_store_b32(1u, rs, lane0 ? off : 0x7FFFFF00u, base, 16); }
DEV bool gather_m(rsrc_t rs, unsigned base, unsigned off0, unsigned off1, u4& f0, u4& f1, int& cap, int& sp) {
    sp = 0;
    for (;;) {
        asm volatile("" ::: "memory");
        f0 = ld_sc1(rs, off0, base);
        f1 = ld_sc1(rs, off1, base);
        const unsigned m = umax3(umax3(f0.x, f0.y, f0.z), umax3(f0.w, f1.x, f1.y), umax3(f1.z, f1.w, 0u));
        ++sp;
        if (!__any(m == SENT)) return true;
        if (sp > cap) { cap = 0; return false; }
    }
}

DEV void halfsum4(const f4& a, const f4& b, float* x) {
#pragma unroll
    for (int r = 0; r < 4; ++r) {
        auto sw = __builtin_amdgcn_permlane32_swap(__float_as_uint(a[r]), __float_as_uint(b[r]), false, false);
        x[r] = __uint_as_float(sw[0]) + __uint_as_float(sw[1]);
    }
}
DEV void st2_sc1(u2 v, rsrc_t rs, unsigned voff, unsigned soff) { __builtin_amdgcn_raw_buffer_store_b64(v, rs, voff, soff, 16); }
DEV u2 ld2_sc1(rsrc_t rs, unsigned voff, unsigned soff) { return __builtin_amdgcn_raw_buffer_load_b64(rs, voff, soff, 16); }
DEV unsigned srec(int h, unsigned off) { const unsigned rec = (unsigned)h * 1024u + off; return 16384u + (rec & 8191u) + ((rec >> 13) << 14); }
constexpr unsigned OOB = 0x7FFFFF00u;

constexpr unsigned LM = 0, LK = 7168, LR = 7168;
template <int NT, bool S2W>
DEV void score_wave(const RecArgs& a, int k) {
    char* ws = a.ws;
    const int tid = threadIdx.x, lane = tid & 63, w = __builtin_amdgcn_readfirstlane(tid >> 6);
    const int l15 = lane & 15, kg = lane >> 4, h = k >> 1, p = k & 1;
    const rsrc_t rs = __builtin_amdgcn_make_buffer_rsrc(ws + OFF_HOP, 0, 8u << 20, 0x00020000);
    const rsrc_t rw = __builtin_amdgcn_make_buffer_rsrc(ws, 0, (unsigned)WS_END, 0x00020000);
    const int idx = (lane & 8) ? 0xEEEE : 0x4444;
    const unsigned off0 = (2 * w) * 1024 + lane * 16, off1 = ((2 * w + 1) % 7) * 1024 + lane * 16;
    unsigned la[5];
#pragma unroll
    for (int j = 0; j < 5; ++j) la[j] = LM + ((2 * w + 2 + j) % 7) * 1024 + lane * 16;
    unsigned* tmo = (unsigned*)(ws + OFF_CTL);
    int cap = SPIN_CAP;
    const bool lo8 = !(lane & 8);
    const int i8 = lane & 7;
    const int tl0 = w < 2 ? 4 * w : 8 + 3 * (w - 2);
    bf16v wa[NT][7];
#pragma unroll
    for (int i = 0; i < NT; ++i)
#pragma unroll
        for (int j = 0; j < 7; ++j) {
            const char* q = ws + OFF_WAF + (((size_t)h * 28 + 14 * p + tl0 + i) * 7 + (2 * w + j) % 7) * 2048 + lane * 16;
            wa[i][j] = join16(*(const u4*)q, *(const u4*)(q + 1024));
            asm volatile("" : "+a"(wa[i][j]));
        }
    const unsigned kwaddr = LK + (16 * tl0 + 16 * (lane >> 5) + l15) * 16 + ((lane >> 4) & 1) * 8;
    const unsigned kwaddr1 = LK + (16 * (tl0 + 2) + l15) * 16 + ((lane >> 4) & 1) * 8;
    const unsigned mdense = LM + (7 * p) * 512 + (kg * 8 + i8) * 16;
    const unsigned traddr = LK + ((8 * kg + (l15 >> 2)) * 16 + (l15 & 3) * 8);
    const unsigned kmoff = (unsigned)OFF_KM + (h * 14 + 7 * p) * 512 + (kg * 8 + i8) * 16;
    const unsigned pso = (lo8 && kg < 2) ? srec(h, 640 + 128 * p + (i8 * 2 + kg) * 8) : OOB;
    int pol = a.layer ? read_pol(a, rw, cap) : 0;
    bool allok = true;
    auto step = [&](int s, auto lastc) {
        constexpr bool LAST = decltype(lastc)::value;
        if (s == POL_SW - 1 && a.layer == 0) pol = read_pol(a, rw, cap);
        const unsigned base = hop_base(a.layer, pol, s);
        u4 f0, f1;
        f4 acc[4];
#pragma unroll
        for (int i = 0; i < NT; ++i) { acc[i] = f4{0, 0, 0, 0}; PINV(acc[i]); }
        if (s > 0) {
            if (w == 2) {
                wait_canary(rs, CAN_M, base, cap);
                delay16(a.dA);
            }
            __builtin_amdgcn_s_barrier();
        }
        int np;
        const bool ok = gather_m(rs, base, off0, off1, f0, f1, cap, np);
        lds_st(LM + off0, f0); lds_st(LM + off1, f1);
        lds_barrier();
        u4 m2 = lds_ld(la[0]), m3 = lds_ld(la[1]), m4 = lds_ld(la[2]), m5 = lds_ld(la[3]), m6 = lds_ld(la[4]);
        u4 md[7];
        if (S2W) {
#pragma unroll
            for (int i = 0; i < 7; ++i) md[i] = lds_ld(mdense + i * 512);
        }
        SB();
        auto pairepi = [&](int pr) {
            float x[4];
            halfsum4(acc[2 * pr], acc[2 * pr + 1], x);
            *(LAS u2*)(size_t)(kwaddr + pr * 512) = u2{pk(x[0], x[1]), pk(x[2], x[3])};
        };
#pragma unroll
        for (int i = 0; i < NT; ++i) SMF(acc[i], f0, wa[i][0], idx);
#pragma unroll
        for (int i = 0; i < NT; ++i) SMF(acc[i], f1, wa[i][1], idx);
        SMF(acc[0], m2, wa[0][2], idx); SMF(acc[1], m2, wa[1][2], idx); SMF(acc[0], m3, wa[0][3], idx); SMF(acc[1], m3, wa[1][3], idx);
        SMF(acc[0], m4, wa[0][4], idx); SMF(acc[1], m4, wa[1][4], idx); SMF(acc[0], m5, wa[0][5], idx); SMF(acc[1], m5, wa[1][5], idx);
        SMF(acc[0], m6, wa[0][6], idx); SMF(acc[1], m6, wa[1][6], idx);
        if constexpr (NT == 4) {
            SMF(acc[2], m2, wa[2][2], idx); SMF(acc[3], m2, wa[3][2], idx); SMF(acc[2], m3, wa[2][3], idx); SMF(acc[3], m3, wa[3][3], idx);
            SMF(acc[2], m4, wa[2][4], idx); SMF(acc[3], m4, wa[3][4], idx);
            asm volatile("s_nop 1" : "+v"(acc[0]), "+v"(acc[1]));
            SB();
            SMF(acc[2], m5, wa[2][5], idx); SMF(acc[3], m5, wa[3][5], idx); SMF(acc[2], m6, wa[2][6], idx); SMF(acc[3], m6, wa[3][6], idx);
            pairepi(0);
            SB();
            asm volatile("s_nop 7" : "+v"(acc[2]), "+v"(acc[3]));
            pairepi(1);
        } else {
            SMF(acc[2], m2, wa[2][2], idx); SMF(acc[2], m3, wa[2][3], idx); SMF(acc[2], m4, wa[2][4], idx);
            asm volatile("s_nop 4" : "+v"(acc[0]), "+v"(acc[1]));
            SB();
            SMF(acc[2], m5, wa[2][5], idx); SMF(acc[2], m6, wa[2][6], idx);
            pairepi(0);
            SB();
            asm volatile("s_nop 7" : "+v"(acc[2]));
            float x[4];
            halfsum4(acc[2], acc[2], x);
            *(LAS u2*)(size_t)kwaddr1 = u2{pk(x[0], x[1]), pk(x[2], x[3])};
        }
        lds_barrier();
        if (S2W) {
            bf4 klo[7], khi[7];
#pragma unroll
            for (int i = 0; i < 7; ++i) {
                klo[i] = __builtin_amdgcn_ds_read_tr16_b64_v4bf16((LAS bf4*)(size_t)(traddr + i * 512));
                khi[i] = __builtin_amdgcn_ds_read_tr16_b64_v4bf16((LAS bf4*)(size_t)(traddr + i * 512 + 64));
                SB();
            }
            u4 kf[7];
#pragma unroll
            for (int i = 0; i < 7; ++i) { bf8 t = {klo[i][0], klo[i][1], klo[i][2], klo[i][3], khi[i][0], khi[i][1], khi[i][2], khi[i][3]}; kf[i] = __builtin_bit_cast(u4, t); }
            f4 d2;
            MF0(d2, kf[0], md[0]);
#pragma unroll
            for (int i = 1; i < 7; ++i) MF(d2, kf[i], md[i]);
            asm volatile("s_nop 7" : "+v"(d2));
            if (!LAST) st2_sc1(u2{nosent(pk(d2[0], d2[1])), nosent(pk(d2[2], d2[3]))}, rs, pso, base);
            SB();
            if (s > 0 && lane == 0) add_flag(ws, flag_off(a.layer, FKM, s - 1));
            if (lo8) {
#pragma unroll
                for (int i = 0; i < 7; ++i) st_sc1(kf[i], rw, kmoff + i * 512, s * (16 * 14 * 512));
            }
            if (LAST) { drain(); if (lane == 0) add_flag(ws, flag_off(a.layer, FKM, s)); }
        }
        allok = allok && ok;
    };
    for (int s = 0; s < NSEG - 1; ++s) step(s, BoolC<false>());
    step(NSEG - 1, BoolC<true>());
    if (!allok && lane == 0) atomicAdd(tmo, 1u);
}
DEV void score_body(const RecArgs& a, int k) {
    const int w = __builtin_amdgcn_readfirstlane(threadIdx.x >> 6);
    if (w < 2) score_wave<4, false>(a, k); else if (w == 2) score_wave<3, false>(a, k); else score_wave<3, true>(a, k);
}

#define SPARSE4(acc, wt, f0, f1, la, idx, EPI0) do {                                                  \
        u4 m2 = lds_ld(la[0]), m3 = lds_ld(la[1]), m4 = lds_ld(la[2]), m5 = lds_ld(la[3]), m6 = lds_ld(la[4]); \
        SB();                                                                                        \
        SMF(acc[0], f0, wt[0][0], idx); SMF(acc[1], f0, wt[1][0], idx); SMF(acc[2], f0, wt[2][0], idx); SMF(acc[3], f0, wt[3][0], idx); \
        SMF(acc[0], f1, wt[0][1], idx); SMF(acc[1], f1, wt[1][1], idx); SMF(acc[2], f1, wt[2][1], idx); SMF(acc[3], f1, wt[3][1], idx); \
        SMF(acc[0], m2, wt[0][2], idx); SMF(acc[1], m2, wt[1][2], idx); SMF(acc[0], m3, wt[0][3], idx); SMF(acc[1], m3, wt[1][3], idx); \
        SMF(acc[0], m4, wt[0][4], idx); SMF(acc[1], m4, wt[1][4], idx); SMF(acc[0], m5, wt[0][5], idx); SMF(acc[1], m5, wt[1][5], idx); \
        SMF(acc[0], m6, wt[0][6], idx); SMF(acc[1], m6, wt[1][6], idx);                              \
        SMF(acc[2], m2, wt[2][2], idx); SMF(acc[3], m2, wt[3][2], idx); SMF(acc[2], m3, wt[2][3], idx); SMF(acc[3], m3, wt[3][3], idx); \
        SMF(acc[2], m4, wt[2][4], idx); SMF(acc[3], m4, wt[3][4], idx);                              \
        asm volatile("s_nop 1" : "+v"(acc[0]), "+v"(acc[1]));        \
        SB();                                                                                        \
        SMF(acc[2], m5, wt[2][5], idx); SMF(acc[3], m5, wt[3][5], idx); SMF(acc[2], m6, wt[2][6], idx); SMF(acc[3], m6, wt[3][6], idx); \
        EPI0;                                                                                        \
        SB();                                                                                        \
        asm volatile("s_nop 7" : "+v"(acc[2]), "+v"(acc[3]));                                        \
    } while (0)
DEV void value_body(const RecArgs& a, int c) {
    char* ws = a.ws;
    const int tid = threadIdx.x, lane = tid & 63, w = __builtin_amdgcn_readfirstlane(tid >> 6);
    const int kg = lane >> 4;
    const rsrc_t rs = __builtin_amdgcn_make_buffer_rsrc(ws + OFF_HOP, 0, 8u << 20, 0x00020000);
    const rsrc_t rw = __builtin_amdgcn_make_buffer_rsrc(ws, 0, (unsigned)WS_END, 0x00020000);
    const u4 srdw = make_srd(ws, (unsigned)WS_END);
    const int idx = (lane & 8) ? 0xEEEE : 0x4444;
    const unsigned off0 = (2 * w) * 1024 + lane * 16, off1 = ((2 * w + 1) % 7) * 1024 + lane * 16;
    unsigned la[5];
#pragma unroll
    for (int j = 0; j < 5; ++j) la[j] = LM + ((2 * w + 2 + j) % 7) * 1024 + lane * 16;
    unsigned* tmo = (unsigned*)(ws + OFF_CTL);
    int cap = SPIN_CAP;
    bf16v wt[4][7];
#pragma unroll
    for (int i = 0; i < 4; ++i)
#pragma unroll
        for (int j = 0; j < 7; ++j) {
            const char* p = ws + OFF_WBF + (((size_t)c * 16 + 4 * w + i) * 7 + (2 * w + j) % 7) * 2048 + lane * 16;
            wt[i][j] = join16(*(const u4*)p, *(const u4*)(p + 1024));
            asm volatile("" : "+a"(wt[i][j]));
        }
    const int hi8 = (lane >> 3) & 1, i8 = lane & 7;
    const unsigned oE0 = srec(4 * w + 2 * hi8, (i8 * 4 + kg) * 16), oE1 = oE0 + 1024;
    const int hs = 4 * w + 2 * hi8 + (kg >> 1);
    const unsigned oST = srec(hs, 512 + i8 * 16), oP0 = srec(hs, 640 + (i8 * 2 + (kg & 1)) * 8), oP1 = oP0 + 128;
    const unsigned vsoff = (unsigned)OFF_VS + (c * 16 + 4 * w) * 1024 + lane * 16;
    const unsigned vmoff = (unsigned)OFF_VM + (c * 4 + w) * 1024 + lane * 16;
    const unsigned lrw = LR + w * 512 + (kg * 8 + i8) * 16;
    const int ep = lane & 3, ii = (lane >> 2) & 7, hf = lane >> 5;
    const unsigned lrr = LR + (((2 * hf + (ep >> 1)) * 8 + ii) * 4 + 2 * (ep & 1)) * 4;
    const unsigned mo = ep == 0 ? (unsigned)c * 256u + hf * 128u + ii * 16u : OOB;
    u4 vs[4];
    int pol = a.layer ? read_pol(a, rw, cap) : 0;
    bool allok = wait_flag(rw, flag_off(a.layer, F12V, 0), 56, cap);
#pragma unroll
    for (int i = 0; i < 4; ++i) vs[i] = ld_sc1(rw, vsoff + i * 1024, 0);
    unsigned fl = 0;
    int D2 = a.dB2, hits = 0;
    auto step = [&](int s, auto lastc) {
        constexpr bool LAST = decltype(lastc)::value;
        if (s == POL_SW - 1 && a.layer == 0) pol = read_pol(a, rw, cap);
        const unsigned base = hop_base(a.layer, pol, s);
        unsigned nb = hop_base(a.layer, pol, s + 1); PINS(nb);
        u4 f0, f1;
        f4 acc[4];
#pragma unroll
        for (int i = 0; i < 4; ++i) { acc[i] = f4{0, 0, 0, 0}; PINV(acc[i]); }
        if (s > 0) delay16(a.dB1);
        int np;
        bool ok = gather_m(rs, base, off0, off1, f0, f1, cap, np);
        if constexpr (LAST) asm volatile("" :: "v"(vs[0]), "v"(vs[1]), "v"(vs[2]), "v"(vs[3]));
        lds_st(LM + off0, f0); lds_st(LM + off1, f1);
        lds_barrier();
        if (!LAST && s > 0) {
            if (__builtin_amdgcn_readfirstlane(fl) < 56) ok = wait_flag(rw, flag_off(a.layer, F12V, s), 56, cap) && ok;
            const unsigned so2 = s * (28 * 16 * 1024);
            PREF(vs[0], vsoff, srdw, so2, 0); PREF(vs[1], vsoff, srdw, so2, 1024); PREF(vs[2], vsoff, srdw, so2, 2048); PREF(vs[3], vsoff, srdw, so2, 3072);
        }
        if (!LAST) fl = ld_flag(rw, flag_off(a.layer, F12V, s + 1));
        if (s > 0 && w == 1 && lane == 0) add_flag(ws, flag_off(a.layer, FVM, s - 1));
        u4 vm;
        {
            float x[4], y[4];
            SPARSE4(acc, wt, f0, f1, la, idx, halfsum4(acc[0], acc[1], x));
            halfsum4(acc[2], acc[3], y);
            vm = u4{pk(x[0], x[1]), pk(x[2], x[3]), pk(y[0], y[1]), pk(y[2], y[3])};
            PINV(vm);
        }
        if constexpr (LAST) {
            st_sc1(vm, rw, vmoff, s * (28 * 4 * 1024));
            drain(); lds_barrier();
            if (w == 1 && lane == 0) add_flag(ws, flag_off(a.layer, FVM, s));
            allok = allok && ok; return;
        } else {
            delay16(D2);
            bool missed = false;
            u4 e0, e1; u2 st, p0, p1;
            p0 = ld2_sc1(rs, oP0, base);
            p1 = ld2_sc1(rs, oP1, base);
            st = ld2_sc1(rs, oST, base);
            e0 = ld_sc1(rs, oE0, base);
            e1 = ld_sc1(rs, oE1, base);
            auto regather = [&]() {
                int sp = 0;
                missed = true;
                for (;;) {
                    asm volatile("" ::: "memory");
                    p0 = ld2_sc1(rs, oP0, base);
                    p1 = ld2_sc1(rs, oP1, base);
                    st = ld2_sc1(rs, oST, base);
                    e0 = ld_sc1(rs, oE0, base);
                    e1 = ld_sc1(rs, oE1, base);
                    const unsigned m = umax3(umax3(umax3(p0.x, p0.y, p1.x), umax3(p1.y, st.x, st.y), umax3(e0.x, e0.y, e0.z)),
                                             umax3(e0.w, e1.x, e1.y), umax3(e1.z, e1.w, 0u));
                    if (!__any(m == SENT)) break;
                    if (++sp > cap) { cap = 0; ok = false; break; }
                }
            };
            if (__builtin_expect(__any(umax3(umax3(p0.x, p0.y, p1.x), umax3(p1.y, st.x, st.y), 0u) == SENT), 0)) regather();
            if (c == 27 && w == 1) put_canary(rs, lane == 0, CAN_M, nb);
            const float s0 = bflo(p0.x) + bflo(p1.x), s1 = bfhi(p0.x) + bfhi(p1.x), s2 = bflo(p0.y) + bflo(p1.y), s3 = bfhi(p0.y) + bfhi(p1.y);
            const float mloc = __uint_as_float(st.x), sloc = __uint_as_float(st.y);
            float M = fmaxf(fmaxf(fmaxf(s0, s1), fmaxf(s2, s3)), mloc);
            M = swap16_partner_max(M);
            float y0 = s0 - M, y1 = s1 - M, y2 = s2 - M, y3 = s3 - M, y4 = mloc - M;
            asm volatile("" : "+v"(y0), "+v"(y1), "+v"(y2), "+v"(y3), "+v"(y4));
            SB();
            if (__builtin_expect(__any(umax3(umax3(umax3(e0.x, e0.y, e0.z), e0.w, e1.x), umax3(e1.y, e1.z, e1.w), 0u) == SENT), 0)) {
                const u2 kp0 = p0, kp1 = p1, kst = st;
                regather();
                p0 = kp0; p1 = kp1; st = kst;
            }
            f4 da0, db0, da1, db1;
            MF0(da0, vs[0], e0); MF0(db0, vs[2], e0); MF0(da1, vs[1], e1); MF0(db1, vs[3], e1);
            SB();
            const float x0 = ex2(y0), x1 = ex2(y1), x2 = ex2(y2), x3 = ex2(y3), f = ex2(y4);
            float l = (x0 + x1) + (x2 + x3);
            l = swap16_partner_add(l);
            l = fmaf(f, sloc, l);
            const float inv = __builtin_amdgcn_rcpf(l);
            const unsigned q0 = pk(x0 * inv, x1 * inv), q1 = pk(x2 * inv, x3 * inv);
            u4 pb;
            pb.x = hi8 ? 0u : q0; pb.y = hi8 ? 0u : q1;
            pb.z = __builtin_amdgcn_mov_dpp(q0, 0x108  , 0xf, 0xf, true);
            pb.w = __builtin_amdgcn_mov_dpp(q1, 0x108, 0xf, 0xf, true);
            f4 o;
            MF0N(o, vm, pb);
            const float scv = f * inv;
            auto sw = __builtin_amdgcn_permlane32_swap(__float_as_uint(scv), __float_as_uint(scv), false, false);
            const float sc0 = __uint_as_float(sw[0]), sc1 = __uint_as_float(sw[1]);
            asm volatile("" : "+v"(da0), "+v"(db0), "+v"(da1), "+v"(db1));
            float t[4];
#pragma unroll
            for (int r = 0; r < 4; ++r) t[r] = sc0 * (hi8 ? db0[r] : da0[r]) + sc1 * (hi8 ? db1[r] : da1[r]);
            asm volatile("s_nop 3" : "+v"(o), "+v"(t[0]), "+v"(t[1]), "+v"(t[2]), "+v"(t[3]));
#pragma unroll
            for (int r = 0; r < 4; ++r) {
                const float v = t[r] + o[r];
                o[r] = v + __uint_as_float(__builtin_amdgcn_mov_dpp(__float_as_uint(v), 0x128  , 0xf, 0xf, true));
            }
            *(LAS f4*)(size_t)lrw = o;
            lds_barrier();
            if (w == 0) {
                typedef float f2 __attribute__((ext_vector_type(2)));
                f2 r0 = *(LAS const f2*)(size_t)lrr, r1 = *(LAS const f2*)(size_t)(lrr + 512), r2 = *(LAS const f2*)(size_t)(lrr + 1024), r3 = *(LAS const f2*)(size_t)(lrr + 1536);
                const unsigned d = nosent(pk((r0[0] + r1[0]) + (r2[0] + r3[0]), (r0[1] + r1[1]) + (r2[1] + r3[1])));
                u4 v;
                v.x = d;
                v.y = __builtin_amdgcn_mov_dpp(d, 0x55  , 0xf, 0xf, true);
                v.z = __builtin_amdgcn_mov_dpp(d, 0xAA, 0xf, 0xf, true);
                v.w = __builtin_amdgcn_mov_dpp(d, 0xFF, 0xf, 0xf, true);
                st_sc1(v, rs, mo, nb);
            }
            SB();
            st_sc1(vm, rw, vmoff, s * (28 * 4 * 1024));
            allok = allok && ok;
            if (missed) { D2 = D2 + A2_UP < 28 ? D2 + A2_UP : 28; hits = 0; }
            else if (++hits >= A2_RUN) { hits = 0; D2 = D2 > 4 ? D2 - 1 : D2; }
        }
    };
    for (int s = 0; s < NSEG - 1; ++s) step(s, BoolC<false>());
    step(NSEG - 1, BoolC<true>());
    if (!allok && lane == 0) atomicAdd(tmo, 1u);
}

template <int NQ>
DEV void seg_wave(const RecArgs& a, int h) {
    char* ws = a.ws;
    const int tid = threadIdx.x, lane = tid & 63, w = __builtin_amdgcn_readfirstlane(tid >> 6);
    const int kg = lane >> 4, i8 = lane & 7;
    const rsrc_t rs = __builtin_amdgcn_make_buffer_rsrc(ws + OFF_HOP, 0, 8u << 20, 0x00020000);
    const rsrc_t rw = __builtin_amdgcn_make_buffer_rsrc(ws, 0, (unsigned)WS_END, 0x00020000);
    const u4 srdw = make_srd(ws, (unsigned)WS_END);
    unsigned* tmo = (unsigned*)(ws + OFF_CTL);
    int cap = SPIN_CAP;
    const int tq0 = w < 2 ? 4 * w : 8 + 3 * (w - 2);
    const unsigned mo = tq0 * 512 + (kg * 8 + i8) * 16;
    const unsigned ksoff = (unsigned)OFF_KS + h * 28672 + tq0 * 1024 + lane * 16;
    const bool lo8 = !(lane & 8);
    const unsigned so = lo8 ? srec(h, (i8 * 4 + kg) * 16) : kg == 0 ? srec(h, 512 + i8 * 16) : OOB;
    int pol = a.layer ? read_pol(a, rw, cap) : 0;
    bool allok = wait_flag(rw, flag_off(a.layer, F12K, 0), 56, cap);
    u4 k0[NQ], k1[NQ];
#pragma unroll
    for (int j = 0; j < NQ; ++j) { k0[j] = ld_sc1(rw, ksoff + j * 1024, 0); k1[j] = ld_sc1(rw, ksoff + 14336 + j * 1024, 0); }
    unsigned fl = 0;
    for (int s = 0; s < NSEG - 1; ++s) {
        if (s == POL_SW - 1 && a.layer == 0) pol = read_pol(a, rw, cap);
        const unsigned base = hop_base(a.layer, pol, s);
        if (s > 0) {
            if (w == 3) {
                wait_canary(rs, CAN_M, base, cap);
                delay16(a.dC);
            }
            __builtin_amdgcn_s_barrier();
        }
        u4 md[NQ];
        int sp = 0; bool ok = true;
        for (;;) {
            asm volatile("" ::: "memory");
#pragma unroll
            for (int j = 0; j < NQ; ++j) md[j] = ld_sc1(rs, mo + j * 512, base);
            unsigned m = umax3(umax3(md[0].x, md[0].y, md[0].z), md[0].w, 0u);
#pragma unroll
            for (int j = 1; j < NQ; ++j) m = umax3(umax3(md[j].x, md[j].y, md[j].z), md[j].w, m);
            ++sp;
            if (!__any(m == SENT)) break;
            if (sp > cap) { cap = 0; ok = false; break; }
        }
        f4 d0, d1;
        MF0(d0, k0[0], md[0]); MF0(d1, k1[0], md[0]);
#pragma unroll
        for (int j = 1; j < NQ; ++j) { MF(d0, k0[j], md[j]); MF(d1, k1[j], md[j]); }
        asm volatile("s_nop 7" : "+v"(d0), "+v"(d1));
        if (w > 0) { *(LAS f4*)(size_t)((w - 1) * 2048 + lane * 16) = d0; *(LAS f4*)(size_t)((w - 1) * 2048 + 1024 + lane * 16) = d1; }
        lds_barrier();
        if (w == 0) {
#pragma unroll
            for (int q = 0; q < 3; ++q) { d0 += *(LAS const f4*)(size_t)(q * 2048 + lane * 16); d1 += *(LAS const f4*)(size_t)(q * 2048 + 1024 + lane * 16); }
            float mx = fmaxf(fmaxf(fmaxf(d0[0], d0[1]), fmaxf(d0[2], d0[3])), fmaxf(fmaxf(d1[0], d1[1]), fmaxf(d1[2], d1[3])));
            mx = swap16_partner_max(mx); mx = swap32_partner_max(mx);
            const float e0 = ex2(d0[0] - mx), e1 = ex2(d0[1] - mx), e2 = ex2(d0[2] - mx), e3 = ex2(d0[3] - mx);
            const float e4 = ex2(d1[0] - mx), e5 = ex2(d1[1] - mx), e6 = ex2(d1[2] - mx), e7 = ex2(d1[3] - mx);
            float sm = ((e0 + e1) + (e2 + e3)) + ((e4 + e5) + (e6 + e7));
            sm = swap16_partner_add(sm); sm = swap32_partner_add(sm);
            const u4 ev = {nosent(pk(e0, e1)), nosent(pk(e2, e3)), nosent(pk(e4, e5)), nosent(pk(e6, e7))};
            const unsigned smx = nosent(__float_as_uint(mx)), ssm = nosent(__float_as_uint(sm));
            st_sc1(lo8 ? ev : u4{smx, ssm, smx, ssm}, rs, so, base);
        }
        SB();
        if (s + 2 < NSEG) {
            if (__builtin_amdgcn_readfirstlane(fl) < 56) ok = wait_flag(rw, flag_off(a.layer, F12K, s + 1), 56, cap) && ok;
            const unsigned so2 = (s + 1) * 458752;
            PREF(k0[0], ksoff, srdw, so2, 0); PREF(k0[1], ksoff, srdw, so2, 1024); PREF(k0[2], ksoff, srdw, so2, 2048);
            if constexpr (NQ == 4) PREF(k0[NQ - 1], ksoff, srdw, so2, 3072);
            const unsigned so3 = so2 + 14336;
            PREF(k1[0], ksoff, srdw, so3, 0); PREF(k1[1], ksoff, srdw, so3, 1024); PREF(k1[2], ksoff, srdw, so3, 2048);
            if constexpr (NQ == 4) PREF(k1[NQ - 1], ksoff, srdw, so3, 3072);
            fl = ld_flag(rw, flag_off(a.layer, F12K, s + 2));
        }
        allok = allok && ok;
    }
    if (!allok && lane == 0) atomicAdd(tmo, 1u);
}
DEV void seg_body(const RecArgs& a, int h) {
    const int w = __builtin_amdgcn_readfirstlane(threadIdx.x >> 6);
    if (w < 2) seg_wave<4>(a, h); else seg_wave<3>(a, h);
}

template <bool isK>
DEV void w12_body(const RecArgs& a, int blk) {
    char* ws = a.ws;
    const int tid = threadIdx.x, lane = tid & 63, w = __builtin_amdgcn_readfirstlane(tid >> 6);
    const int wu = blk * 4 + w;
    const int l15 = lane & 15, kg = lane >> 4;
    const rsrc_t rw = __builtin_amdgcn_make_buffer_rsrc(ws, 0, (unsigned)WS_END, 0x00020000);
    u4 wt[2][14];
    unsigned so0, so1, sstride;
    if (isK) {
        const int h = wu / 14, tp = wu % 14;
#pragma unroll
        for (int u = 0; u < 2; ++u) {
            const int aa = 32 * tp + 8 * (l15 >> 2) + 4 * u + (l15 & 3);
#pragma unroll
            for (int ks = 0; ks < 14; ++ks)
                wt[u][ks] = *(const u4*)(ws + OFF_WAF + ((((size_t)h * 28 + (aa >> 4)) * 14 + ks) * 64 + kg * 16 + (aa & 15)) * 16);
        }
        so0 = (unsigned)OFF_KS + (h * 28 + tp) * 1024 + lane * 16; so1 = so0 + 14336; sstride = 458752;
    } else {
        const int idx = wu - 224, h = idx & 15, c0 = 2 * (idx >> 4);
#pragma unroll
        for (int u = 0; u < 2; ++u)
#pragma unroll
            for (int ks = 0; ks < 14; ++ks)
                wt[u][ks] = *(const u4*)(ws + OFF_WBF + ((((size_t)(c0 + u) * 16 + h) * 14 + ks) * 64 + lane) * 16);
        so0 = (unsigned)OFF_VS + (c0 * 16 + h) * 1024 + lane * 16; so1 = so0 + 16384; sstride = 458752;
    }
    const char* xsrc = ws + OFF_XP + w * 7168 + lane * 16;
    int cap = SPIN_CAP; bool ok = true;
    auto stage = [&](int s) {
        if (a.layer) ok = wait_flag(rw, flag_off(a.layer, FXR, s), 2, cap) && ok;
#pragma unroll
        for (int i = 0; i < 7; ++i) glds16_sc1(xsrc + (size_t)s * 28672 + i * 1024, (s & 1) * 28672 + w * 7168 + i * 1024);
    };
    const unsigned fo = flag_off(a.layer, isK ? F12K : F12V, 0);
    stage(0);
    for (int s = 0; s < NSEG; ++s) {
        if (s == 0) asm volatile("s_waitcnt vmcnt(0)" ::: "memory"); else asm volatile("s_waitcnt vmcnt(2)" ::: "memory");
        __builtin_amdgcn_s_barrier();
        asm volatile("" ::: "memory");
        if (s >= 3 && tid == 0) add_flag(ws, fo + (s - 2) * 128);
        if (s + 1 < NSEG) stage(s + 1);
        const unsigned lb = (s & 1) * 28672 + lane * 16;
        f4 c00, c01, c10, c11;
        u4 x0[14], x1[14];
#pragma unroll
        for (int ks = 0; ks < 14; ++ks) { x0[ks] = lds_ld(lb + ks * 1024); x1[ks] = lds_ld(lb + 14336 + ks * 1024); }
        SB();
        if (isK) { MF0(c00, wt[0][0], x0[0]); MF0(c10, wt[1][0], x0[0]); MF0(c01, wt[0][0], x1[0]); MF0(c11, wt[1][0], x1[0]); }
        else { MF0(c00, x0[0], wt[0][0]); MF0(c01, x1[0], wt[0][0]); MF0(c10, x0[0], wt[1][0]); MF0(c11, x1[0], wt[1][0]); }
#pragma unroll
        for (int ks = 1; ks < 14; ++ks) {
            if (isK) { MF(c00, wt[0][ks], x0[ks]); MF(c10, wt[1][ks], x0[ks]); MF(c01, wt[0][ks], x1[ks]); MF(c11, wt[1][ks], x1[ks]); }
            else { MF(c00, x0[ks], wt[0][ks]); MF(c01, x1[ks], wt[0][ks]); MF(c10, x0[ks], wt[1][ks]); MF(c11, x1[ks], wt[1][ks]); }
        }
        asm volatile("s_nop 7" : "+v"(c00), "+v"(c01), "+v"(c10), "+v"(c11));
        u4 v0, v1;
        if (isK) {
            v0 = u4{pk(c00[0], c00[1]), pk(c00[2], c00[3]), pk(c10[0], c10[1]), pk(c10[2], c10[3])};
            v1 = u4{pk(c01[0], c01[1]), pk(c01[2], c01[3]), pk(c11[0], c11[1]), pk(c11[2], c11[3])};
        } else {
            v0 = u4{pk(c00[0], c00[1]), pk(c00[2], c00[3]), pk(c01[0], c01[1]), pk(c01[2], c01[3])};
            v1 = u4{pk(c10[0], c10[1]), pk(c10[2], c10[3]), pk(c11[0], c11[1]), pk(c11[2], c11[3])};
        }
        st_sc1(v0, rw, so0, s * sstride); st_sc1(v1, rw, so1, s * sstride);
        asm volatile("s_waitcnt lgkmcnt(0)" ::: "memory");
        if (s == 0) {
            drain(); __builtin_amdgcn_s_barrier();
            if (tid == 0) add_flag(ws, fo);
        }
    }
    drain(); __builtin_amdgcn_s_barrier();
    if (tid == 0) { add_flag(ws, fo + (NSEG - 2) * 128); add_flag(ws, fo + (NSEG - 1) * 128); }
    if (!ok && lane == 0) atomicAdd((unsigned*)(ws + OFF_CTL), 1u);
}

DEV void w3a_body(const RecArgs& a, int j) {
    char* ws = a.ws;
    const int tid = threadIdx.x, lane = tid & 63, w = __builtin_amdgcn_readfirstlane(tid >> 6);
    const rsrc_t rw = __builtin_amdgcn_make_buffer_rsrc(ws, 0, (unsigned)WS_END, 0x00020000);
    const int l15 = lane & 15, kg = lane >> 4, h = (j & 3) * 4 + w;
    int cap = SPIN_CAP; bool ok = true;
    for (int s = j >> 2; s < NSEG; s += 4) {
        if (w == 0) ok = wait_flag(rw, flag_off(a.layer, F12K, s), 56, cap) && ok;
        __builtin_amdgcn_s_barrier();
        asm volatile("" ::: "memory");
        const unsigned kb = (unsigned)OFF_KS + (s * 16 + h) * 28672 + lane * 16;
        const unsigned mb = (unsigned)OFF_KM + (s * 16 + h) * 7168 + (kg * 8 + (lane & 7)) * 16;
        const unsigned xb = (unsigned)OFF_XN + s * 28672 + lane * 16;
        u4 x0[14], x1[14];
#pragma unroll
        for (int t = 0; t < 14; ++t) { x0[t] = ld_sc1(rw, xb, t * 1024); x1[t] = ld_sc1(rw, xb + 14336, t * 1024); }
        f4 d00, d01, d10, d11, d20, d21;
        {
            u4 k0[14], k1[14];
#pragma unroll
            for (int t = 0; t < 14; ++t) { k0[t] = ld_sc1(rw, kb, t * 1024); k1[t] = ld_sc1(rw, kb + 14336, t * 1024); }
            MF0B(d00, k0[0], x0[0]); MF0B(d01, k0[0], x1[0]); MF0B(d10, k1[0], x0[0]); MF0B(d11, k1[0], x1[0]);
#pragma unroll
            for (int t = 1; t < 14; ++t) { MFB(d00, k0[t], x0[t]); MFB(d01, k0[t], x1[t]); MFB(d10, k1[t], x0[t]); MFB(d11, k1[t], x1[t]); }
        }
        if (w == 0) ok = wait_flag(rw, flag_off(a.layer, FKM, s), 32, cap, s >= NSEG - 2) && ok;
        __builtin_amdgcn_s_barrier();
        asm volatile("" ::: "memory");
        {
            u4 km[14];
#pragma unroll
            for (int t = 0; t < 14; ++t) km[t] = ld_sc1(rw, mb, t * 512);
            MF0B(d20, km[0], x0[0]); MF0B(d21, km[0], x1[0]);
#pragma unroll
            for (int t = 1; t < 14; ++t) { MFB(d20, km[t], x0[t]); MFB(d21, km[t], x1[t]); }
        }
        asm volatile("s_nop 7" : "+v"(d00), "+v"(d01), "+v"(d10), "+v"(d11), "+v"(d20), "+v"(d21));
        const float NEG = -3.0e38f;
#pragma unroll
        for (int qt = 0; qt < 2; ++qt) {
            const f4 a0 = qt ? d01 : d00, a1 = qt ? d11 : d10, a2 = qt ? d21 : d20;
            const int iq = 16 * qt + l15;
            float v[12];
#pragma unroll
            for (int r = 0; r < 4; ++r) {
                v[r] = (4 * kg + r <= iq) ? a0[r] : NEG;
                v[4 + r] = (16 + 4 * kg + r <= iq) ? a1[r] : NEG;
                v[8 + r] = (kg < 2) ? a2[r] : NEG;
            }
            float mx = v[0];
#pragma unroll
            for (int r = 1; r < 12; ++r) mx = fmaxf(mx, v[r]);
            mx = swap16_partner_max(mx); mx = swap32_partner_max(mx);
            float sm = 0;
#pragma unroll
            for (int r = 0; r < 12; ++r) { v[r] = ex2(v[r] - mx); sm += v[r]; }
            sm = swap16_partner_add(sm); sm = swap32_partner_add(sm);
            const float inv = 1.0f / sm;
            u4 ps = {pk(v[0] * inv, v[1] * inv), pk(v[2] * inv, v[3] * inv), pk(v[4] * inv, v[5] * inv), pk(v[6] * inv, v[7] * inv)};
            st_sc1(ps, rw, (unsigned)OFF_PS + ((s * 16 + h) * 2 + qt) * 1024 + lane * 16, 0);
            if (kg < 2) {
                u2 pm = {pk(v[8] * inv, v[9] * inv), pk(v[10] * inv, v[11] * inv)};
                const int hl = h & 3;
                __builtin_amdgcn_raw_buffer_store_b64(pm, rw, (unsigned)OFF_PM + ((s * 4 + (h >> 2)) * 2 + qt) * 1024 + ((2 * (hl & 1) + kg) * 16 + l15) * 16 + 8 * (hl >> 1), 0, 16);
            }
        }
        drain(); __builtin_amdgcn_s_barrier();
        if (tid == 0) add_flag(ws, flag_off(a.layer, FPS, s));
    }
    if (!ok && lane == 0) atomicAdd((unsigned*)(ws + OFF_CTL), 1u);
}

DEV void w3b_body(const RecArgs& a, int j) {
    char* ws = a.ws;
    const int tid = threadIdx.x, lane = tid & 63, w = __builtin_amdgcn_readfirstlane(tid >> 6);
    const rsrc_t rw = __builtin_amdgcn_make_buffer_rsrc(ws, 0, (unsigned)WS_END, 0x00020000);
    const int l15 = lane & 15, kg = lane >> 4, qt = w & 1, c0 = (j % 7) * 4 + (w >> 1) * 2;
    int cap = SPIN_CAP; bool ok = true;
    for (int s = j / 7; s < NSEG; s += 4) {
        if (w == 0) ok = wait_flag(rw, flag_off(a.layer, F12V, s), 56, cap) && ok;
        __builtin_amdgcn_s_barrier();
        asm volatile("" ::: "memory");
        const unsigned vb = (unsigned)OFF_VS + (s * 28 + c0) * 16384 + lane * 16;
        const unsigned mb = (unsigned)OFF_VM + (s * 28 + c0) * 4096 + lane * 16;
        u4 av[2][16];
#pragma unroll
        for (int u = 0; u < 2; ++u)
#pragma unroll
            for (int h = 0; h < 16; ++h) {
                av[u][h] = ld_sc1(rw, vb + u * 16384, h * 1024);
            }
        const unsigned xe = (32 * s + 16 * qt + l15) * DM + 16 * c0 + 4 * kg, xo = (unsigned)OFF_X32 + xe * 4;
        u4 xu0, xu1;
        if (a.layer) { xu0 = ld_sc1(rw, xo, 0); xu1 = ld_sc1(rw, xo + 64, 0); }
        else {
            const u2 b0 = *(const u2*)(a.x0 + xe), b1 = *(const u2*)(a.x0 + xe + 16);
            xu0 = __builtin_bit_cast(u4, f4{bflo(b0.x), bfhi(b0.x), bflo(b0.y), bfhi(b0.y)});
            xu1 = __builtin_bit_cast(u4, f4{bflo(b1.x), bfhi(b1.x), bflo(b1.y), bfhi(b1.y)});
        }
        if (w == 0) {
            ok = wait_flag(rw, flag_off(a.layer, FVM, s), 28, cap, s >= NSEG - 2) && ok;
            ok = wait_flag(rw, flag_off(a.layer, FPS, s), 4, cap, s >= NSEG - 2) && ok;
        }
        __builtin_amdgcn_s_barrier();
        asm volatile("" ::: "memory");
        u4 ps[16], pm[4], am[2][4];
#pragma unroll
        for (int h = 0; h < 16; ++h) {
            ps[h] = ld_sc1(rw, (unsigned)OFF_PS + ((s * 16 + h) * 2 + qt) * 1024 + lane * 16, 0);
        }
#pragma unroll
        for (int g = 0; g < 4; ++g) {
            pm[g] = ld_sc1(rw, (unsigned)OFF_PM + ((s * 4 + g) * 2 + qt) * 1024 + lane * 16, 0);
            am[0][g] = ld_sc1(rw, mb, g * 1024); am[1][g] = ld_sc1(rw, mb + 4096, g * 1024);
        }
        f4 d0, d1;
        MF0A(d0, av[0][0], ps[0]); MF0A(d1, av[1][0], ps[0]);
#pragma unroll
        for (int h = 1; h < 16; ++h) { MFA(d0, av[0][h], ps[h]); MFA(d1, av[1][h], ps[h]); }
#pragma unroll
        for (int g = 0; g < 4; ++g) { MF(d0, am[0][g], pm[g]); MF(d1, am[1][g], pm[g]); }
        asm volatile("s_nop 7" : "+v"(d0), "+v"(d1));
        st_sc1(__builtin_bit_cast(u4, __builtin_bit_cast(f4, xu0) + d0), rw, xo, 0);
        st_sc1(__builtin_bit_cast(u4, __builtin_bit_cast(f4, xu1) + d1), rw, xo + 64, 0);
        drain(); __builtin_amdgcn_s_barrier();
        if (tid == 0) add_flag(ws, flag_off(a.layer, FXO, s));
    }
    if (!ok && lane == 0) atomicAdd((unsigned*)(ws + OFF_CTL), 1u);
}

DEV void b3_unit(const RecArgs& a, int tile) {
    char* ws = a.ws;
    const int tid = threadIdx.x, lane = tid & 63, w = __builtin_amdgcn_readfirstlane(tid >> 6);
    const rsrc_t rw = __builtin_amdgcn_make_buffer_rsrc(ws, 0, (unsigned)WS_END, 0x00020000);
    typedef float f2 __attribute__((ext_vector_type(2)));
    constexpr unsigned XR = 0, XB = 16 * XLD * 4;
    const int l15 = lane & 15, kg = lane >> 4, tok = tile * 16 + l15;
    u4 oa[4], ob[4]; f2 sa[4][2], sb[4][2];
#pragma unroll
    for (int k = 0; k < 4; ++k) {
        const int i = tid + 256 * k < 896 ? tid + 256 * k : 895, r = i / 56, c8 = (i % 56) * 8, tk = tile * 16 + r;
        oa[k] = *(const u4*)((const u16*)(ws + OFF_BO) + (size_t)tk * DM + c8);
        ob[k] = *(const u4*)((const u16*)(ws + OFF_BO) + ((size_t)SEQ + tk) * DM + c8);
#pragma unroll
        for (int e = 0; e < 2; ++e) {
            const int h = (c8 + 4 * e) / 28;
            sa[k][e] = *(const f2*)(ws + OFF_BS + ((size_t)h * SEQ + tk) * 8); sb[k][e] = *(const f2*)(ws + OFF_BS + ((size_t)(16 + h) * SEQ + tk) * 8);
        }
    }
    u4 wf[4][14];
#pragma unroll
    for (int ti = 0; ti < 4; ++ti)
#pragma unroll
        for (int ks = 0; ks < 14; ++ks) wf[ti][ks] = *(const u4*)(ws + OFF_WOF + ((size_t)(w * 7 + ti) * 14 + ks) * 1024 + lane * 16);
    const unsigned xo = (unsigned)OFF_X32 + (tok * DM + 16 * (w * 7) + 4 * kg) * 4;
    f4 xv[7];
#pragma unroll
    for (int ti = 0; ti < 7; ++ti) xv[ti] = *(const f4*)(ws + xo + ti * 64);
#pragma unroll
    for (int k = 0; k < 4; ++k) {
        const int i = tid + 256 * k;
        if (i < 896) {
            const int r = i / 56, c8 = (i % 56) * 8;
            float wa[2], wb[2];
#pragma unroll
            for (int e = 0; e < 2; ++e) {
                const float M = fmaxf(sa[k][e][0], sb[k][e][0]);
                const float la = sa[k][e][1] * ex2(sa[k][e][0] - M), lb = sb[k][e][1] * ex2(sb[k][e][0] - M);
                const float inv = 1.0f / (la + lb);
                wa[e] = la * inv; wb[e] = lb * inv;
            }
            const u4 p = oa[k], q = ob[k];
            u4 o;
            o.x = pk(wa[0] * bflo(p.x) + wb[0] * bflo(q.x), wa[0] * bfhi(p.x) + wb[0] * bfhi(q.x));
            o.y = pk(wa[0] * bflo(p.y) + wb[0] * bflo(q.y), wa[0] * bfhi(p.y) + wb[0] * bfhi(q.y));
            o.z = pk(wa[1] * bflo(p.z) + wb[1] * bflo(q.z), wa[1] * bfhi(p.z) + wb[1] * bfhi(q.z));
            o.w = pk(wa[1] * bflo(p.w) + wb[1] * bflo(q.w), wa[1] * bfhi(p.w) + wb[1] * bfhi(q.w));
            lds_st(XB + (r * 456 + c8) * 2, o);
        }
    }
    lds_barrier();
    u4 xf[14];
#pragma unroll
    for (int ks = 0; ks < 14; ++ks) xf[ks] = lds_ld(XB + (l15 * 456 + 32 * ks + 8 * kg) * 2);
#pragma unroll
    for (int ti = 0; ti < 7; ++ti) {
        f4 d;
        MF0A(d, wf[ti & 3][0], xf[0]);
#pragma unroll
        for (int ks = 1; ks < 14; ++ks) MFA(d, wf[ti & 3][ks], xf[ks]);
        if (ti < 3) {
#pragma unroll
            for (int ks = 0; ks < 14; ++ks) wf[ti][ks] = *(const u4*)(ws + OFF_WOF + ((size_t)(w * 7 + ti + 4) * 14 + ks) * 1024 + lane * 16);
        }
        asm volatile("s_nop 7" : "+v"(d));
        const f4 v = xv[ti] + d;
        st_sc1(__builtin_bit_cast(u4, v), rw, xo + ti * 64, 0);
        *(LAS f4*)(size_t)(XR + (l15 * XLD + 16 * (w * 7 + ti) + 4 * kg) * 4) = v;
    }
    lds_barrier();
    ln16(XR, XB, tid);
    lds_barrier();
    {
        const unsigned fo = (unsigned)tile * 14336 + lane * 16;
        for (int f = w; f < 14; f += 4) {
            st_sc1(lds_ld(XB + (l15 * 456 + 32 * f + 8 * kg) * 2), rw, (unsigned)OFF_XN + fo, f * 1024);
            const int t = f >> 1, R = f & 1;
            const unsigned base = XB + (l15 * 456 + 64 * t + 16 * (2 * R + (kg >> 1))) * 2 + 8 * (kg & 1);
            const LAS unsigned* p = (const LAS unsigned*)(size_t)base;
            st_sc1(u4{p[0], p[4], p[1], p[5]}, rw, (unsigned)OFF_XP + fo, f * 1024);
        }
    }
    drain(); lds_barrier();
    if (tid == 0) add_flag(ws, flag_off(a.layer, FXR, tile >> 1));
}

DEV void b3q_unit(const RecArgs& a, int tile, int nq) {
    char* ws = a.ws;
    const int tid = threadIdx.x, lane = tid & 63, w = __builtin_amdgcn_readfirstlane(tid >> 6);
    const rsrc_t rw = __builtin_amdgcn_make_buffer_rsrc(ws, 0, (unsigned)WS_END, 0x00020000);
    typedef float f2 __attribute__((ext_vector_type(2)));
    constexpr unsigned XB = 0;
    const int l15 = lane & 15, kg = lane >> 4, tok = tile * 16 + l15;
    const int T0 = nq * 7 + 2 * w, T1 = w < 3 ? T0 + 1 : T0;
    u4 oa[4], ob[4]; f2 sa[4][2], sb[4][2];
#pragma unroll
    for (int k = 0; k < 4; ++k) {
        const int i = tid + 256 * k < 896 ? tid + 256 * k : 895, r = i / 56, c8 = (i % 56) * 8, tk = tile * 16 + r;
        oa[k] = *(const u4*)((const u16*)(ws + OFF_BO) + (size_t)tk * DM + c8);
        ob[k] = *(const u4*)((const u16*)(ws + OFF_BO) + ((size_t)SEQ + tk) * DM + c8);
#pragma unroll
        for (int e = 0; e < 2; ++e) {
            const int h = (c8 + 4 * e) / 28;
            sa[k][e] = *(const f2*)(ws + OFF_BS + ((size_t)h * SEQ + tk) * 8); sb[k][e] = *(const f2*)(ws + OFF_BS + ((size_t)(16 + h) * SEQ + tk) * 8);
        }
    }
    u4 wf[2][14];
#pragma unroll
    for (int ks = 0; ks < 14; ++ks) {
        wf[0][ks] = *(const u4*)(ws + OFF_WOF + ((size_t)T0 * 14 + ks) * 1024 + lane * 16);
        wf[1][ks] = *(const u4*)(ws + OFF_WOF + ((size_t)T1 * 14 + ks) * 1024 + lane * 16);
    }
    const unsigned xo0 = (unsigned)OFF_X32 + (tok * DM + 16 * T0 + 4 * kg) * 4, xo1 = (unsigned)OFF_X32 + (tok * DM + 16 * T1 + 4 * kg) * 4;
    const f4 xv0 = *(const f4*)(ws + xo0), xv1 = *(const f4*)(ws + xo1);
#pragma unroll
    for (int k = 0; k < 4; ++k) {
        const int i = tid + 256 * k;
        if (i < 896) {
            const int r = i / 56, c8 = (i % 56) * 8;
            float wa[2], wb[2];
#pragma unroll
            for (int e = 0; e < 2; ++e) {
                const float M = fmaxf(sa[k][e][0], sb[k][e][0]);
                const float la = sa[k][e][1] * ex2(sa[k][e][0] - M), lb = sb[k][e][1] * ex2(sb[k][e][0] - M);
                const float inv = 1.0f / (la + lb);
                wa[e] = la * inv; wb[e] = lb * inv;
            }
            const u4 p = oa[k], q = ob[k];
            u4 o;
            o.x = pk(wa[0] * bflo(p.x) + wb[0] * bflo(q.x), wa[0] * bfhi(p.x) + wb[0] * bfhi(q.x));
            o.y = pk(wa[0] * bflo(p.y) + wb[0] * bflo(q.y), wa[0] * bfhi(p.y) + wb[0] * bfhi(q.y));
            o.z = pk(wa[1] * bflo(p.z) + wb[1] * bflo(q.z), wa[1] * bfhi(p.z) + wb[1] * bfhi(q.z));
            o.w = pk(wa[1] * bflo(p.w) + wb[1] * bflo(q.w), wa[1] * bfhi(p.w) + wb[1] * bfhi(q.w));
            lds_st(XB + (r * 456 + c8) * 2, o);
        }
    }
    lds_barrier();
    u4 xf[14];
#pragma unroll
    for (int ks = 0; ks < 14; ++ks) xf[ks] = lds_ld(XB + (l15 * 456 + 32 * ks + 8 * kg) * 2);
    f4 d0, d1;
    MF0A(d0, wf[0][0], xf[0]); MF0A(d1, wf[1][0], xf[0]);
#pragma unroll
    for (int ks = 1; ks < 14; ++ks) { MFA(d0, wf[0][ks], xf[ks]); MFA(d1, wf[1][ks], xf[ks]); }
    asm volatile("s_nop 7" : "+v"(d0), "+v"(d1));
    st_sc1(__builtin_bit_cast(u4, xv0 + d0), rw, xo0, 0);
    if (w < 3) st_sc1(__builtin_bit_cast(u4, xv1 + d1), rw, xo1, 0);
    drain(); lds_barrier();
    if (tid == 0) add_flag(ws, flag_off(a.layer, FXQ, tile));
}
DEV void b3ln_unit(const RecArgs& a, int tile) {
    char* ws = a.ws;
    const int tid = threadIdx.x, lane = tid & 63, w = __builtin_amdgcn_readfirstlane(tid >> 6);
    const rsrc_t rw = __builtin_amdgcn_make_buffer_rsrc(ws, 0, (unsigned)WS_END, 0x00020000);
    constexpr unsigned XR = 0, XB = 16 * XLD * 4;
    const int l15 = lane & 15, kg = lane >> 4;
    int cap = SPIN_CAP; bool ok = true;
    if (w == 0) ok = wait_flag(rw, flag_off(a.layer, FXQ, tile), 4, cap, true);
    lds_barrier();
    {
        u4 xin[7];
#pragma unroll
        for (int k = 0; k < 7; ++k) xin[k] = ld_sc1(rw, (unsigned)OFF_X32 + tile * (16 * DM * 4) + tid * 16, k * 4096);
#pragma unroll
        for (int k = 0; k < 7; ++k) { const int i = tid + 256 * k, r = i / 112, c = (i % 112) * 4; lds_st(XR + (r * XLD + c) * 4, xin[k]); }
    }
    lds_barrier();
    ln16(XR, XB, tid);
    lds_barrier();
    {
        const unsigned fo = (unsigned)tile * 14336 + lane * 16;
        for (int f = w; f < 14; f += 4) {
            st_sc1(lds_ld(XB + (l15 * 456 + 32 * f + 8 * kg) * 2), rw, (unsigned)OFF_XN + fo, f * 1024);
            const int t = f >> 1, R = f & 1;
            const unsigned base = XB + (l15 * 456 + 64 * t + 16 * (2 * R + (kg >> 1))) * 2 + 8 * (kg & 1);
            const LAS unsigned* p = (const LAS unsigned*)(size_t)base;
            st_sc1(u4{p[0], p[4], p[1], p[5]}, rw, (unsigned)OFF_XP + fo, f * 1024);
        }
    }
    drain(); lds_barrier();
    if (tid == 0) add_flag(ws, flag_off(a.layer, FXR, tile >> 1));
    if (!ok && lane == 0) atomicAdd((unsigned*)(ws + OFF_CTL), 1u);
}

DEV void b1_unit(const RecArgs& a, int tile, int nq, int& cap, bool& ok) {
    char* ws = a.ws;
    const int tid = threadIdx.x, lane = tid & 63, w = __builtin_amdgcn_readfirstlane(tid >> 6);
    const rsrc_t rw = __builtin_amdgcn_make_buffer_rsrc(ws, 0, (unsigned)WS_END, 0x00020000);
    constexpr unsigned XR = 0, XB = 16 * XLD * 4;
    const int l15 = lane & 15, kg = lane >> 4, T0 = nq * 8 + w * 2, s = tile >> 1;
    u4 wf[2][14];
#pragma unroll
    for (int ti = 0; ti < 2; ++ti)
#pragma unroll
        for (int ks = 0; ks < 14; ++ks) wf[ti][ks] = *(const u4*)(ws + OFF_WQF + ((size_t)(T0 + ti) * 14 + ks) * 1024 + lane * 16);
    if (w == 0) ok = wait_flag(rw, flag_off(a.layer, FXO, s), 7, cap, s >= NSEG - 2) && ok;
    lds_barrier();
    {
        u4 xin[7];
#pragma unroll
        for (int k = 0; k < 7; ++k) xin[k] = ld_sc1(rw, (unsigned)OFF_X32 + tile * (16 * DM * 4) + tid * 16, k * 4096);
#pragma unroll
        for (int k = 0; k < 7; ++k) { const int i = tid + 256 * k, r = i / 112, c = (i % 112) * 4; lds_st(XR + (r * XLD + c) * 4, xin[k]); }
    }
    lds_barrier();
    ln16(XR, XB, tid);
    lds_barrier();
    float kn = 0.f;
    u4 xf[14];
#pragma unroll
    for (int ks = 0; ks < 14; ++ks) xf[ks] = lds_ld(XB + (l15 * 456 + 32 * ks + 8 * kg) * 2);
    const int tok = tile * 16 + l15;
#pragma unroll
    for (int ti = 0; ti < 2; ++ti) {
        const int T = T0 + ti;
        f4 d;
        if (T < 30) {
            MF0A(d, wf[ti][0], xf[0]);
#pragma unroll
            for (int ks = 1; ks < 14; ++ks) MFA(d, wf[ti][ks], xf[ks]);
            asm volatile("s_nop 7" : "+v"(d));
            if (T < 28) {
                const float sc = LOG2E * 0.18898223650461362f;
                const int n = 16 * T + 4 * kg, h = n / 28, dd = n % 28;
                u16* q = (u16*)(ws + OFF_BQ) + ((size_t)h * SEQ + tok) * 32 + dd;
                *(u2*)q = u2{pk(d[0] * sc, d[1] * sc), pk(d[2] * sc, d[3] * sc)};
                if (dd == 24) *(u2*)(q + 4) = u2{0, 0};
            } else {
                const int dd = 16 * (T - 28) + 4 * kg;
                char* kp = ws + OFF_BK + (size_t)(tok >> 5) * 2048 + (dd >> 4) * 1024 + (((dd >> 3) & 1) * 32 + (tok & 31)) * 16 + (dd & 7) * 2;
                const u2 kv = u2{pk(d[0], d[1]), pk(d[2], d[3])};
                *(u2*)kp = kv;
                const float n2 = bflo(kv.x) * bflo(kv.x) + bfhi(kv.x) * bfhi(kv.x) + bflo(kv.y) * bflo(kv.y) + bfhi(kv.y) * bfhi(kv.y);
                kn = T == 28 ? n2 : kn + n2;
                if (T == 29) {
                    float t = swap32_partner_add(swap16_partner_add(kn));
                    t = fmaxf(t, __uint_as_float(__builtin_amdgcn_update_dpp(0u, __float_as_uint(t), 0x128, 0xf, 0xf, false)));
                    t = fmaxf(t, __uint_as_float(__builtin_amdgcn_update_dpp(0u, __float_as_uint(t), 0x124, 0xf, 0xf, false)));
                    t = fmaxf(t, __uint_as_float(__builtin_amdgcn_update_dpp(0u, __float_as_uint(t), 0x122, 0xf, 0xf, false)));
                    t = fmaxf(t, __uint_as_float(__builtin_amdgcn_update_dpp(0u, __float_as_uint(t), 0x121, 0xf, 0xf, false)));
                    if (lane == 0) *(float*)(ws + OFF_BN + tile * 4) = t;
                }
            }
        } else {
            MF0B(d, xf[0], wf[ti][0]);
#pragma unroll
            for (int ks = 1; ks < 14; ++ks) MFB(d, xf[ks], wf[ti][ks]);
            asm volatile("s_nop 7" : "+v"(d));
            const int dd = 16 * (T - 30) + l15;
            if (dd == 28) d = f4{1.f, 1.f, 1.f, 1.f};
            char* vp = ws + OFF_BV + (size_t)(tile >> 1) * 2048 + (tile & 1) * 1024 + ((kg & 1) * 32 + dd) * 16 + 8 * (kg >> 1);
            *(u2*)vp = u2{pk(d[0], d[1]), pk(d[2], d[3])};
        }
    }
    lds_barrier();
}

__global__ void __launch_bounds__(256, 1) k_rec(RecArgs a) {
    const int b = blockIdx.x, x = b & 7, r = b >> 3;
    if (x == 0) score_body(a, r);
    else if (x == 1) { if (r < 28) value_body(a, r); else if (r == 28 && a.layer == 0) probe_body(a); }
    else if (x == 2) { if (r < 16) seg_body(a, r); else if (a.layer == 0) hop_init(1, (r - 16) * 256 + threadIdx.x, 16 * 256, a.mem0, a.ws); }
    else {
        const int wr = r * 5 + (x - 3);
        if (wr < 112) {
            if (wr < 56) w12_body<true>(a, wr); else w12_body<false>(a, wr);
            {
                int cap = SPIN_CAP; bool ok = true;
                for (int u = wr; u < 512; u += 112) b1_unit(a, u >> 2, u & 3, cap, ok);
                if (!ok && (threadIdx.x & 63) == 0) atomicAdd((unsigned*)(a.ws + OFF_CTL), 1u);
            }
        } else {
            if (a.layer) {
                const int j = wr - 112;
                if (j < 16) { b3q_unit(a, j >> 2, j & 3); if ((j & 3) == 0) b3ln_unit(a, j >> 2); for (int t = 36 + j; t < 128; t += 48) b3_unit(a, t); }
                else for (int t = j - 12; t < 128; t += 48) b3_unit(a, t);
            }
            if (wr < 128) w3a_body(a, wr - 112);
            else if (wr < 128 + 28) w3b_body(a, wr - 128);
        }
    }
}

constexpr float THR = 10.0f;
#define MF32C(d, a, b, c) asm volatile("v_mfma_f32_32x32x16_bf16 %0, %1, %2, %3" : "=&v"(d) : "v"(a), "v"(b), "v"(c))
#define MF32(d, a, b) asm volatile("v_mfma_f32_32x32x16_bf16 %0, %1, %2, %0" : "+v"(d) : "v"(a), "v"(b))
#define MF32N(d, a, b) asm volatile("s_nop 1\n\tv_mfma_f32_32x32x16_bf16 %0, %1, %2, %0" : "+v"(d) : "v"(a), "v"(b))
DEV float max16(const f16v& s) {
    float m;
    asm("v_max3_f32 %0, %1, %2, %3\n\tv_max3_f32 %0, %0, %4, %5\n\tv_max3_f32 %0, %0, %6, %7\n\tv_max3_f32 %0, %0, %8, %9\n\t"
        "v_max3_f32 %0, %0, %10, %11\n\tv_max3_f32 %0, %0, %12, %13\n\tv_max3_f32 %0, %0, %14, %15\n\tv_max_f32 %0, %0, %16"
        : "=&v"(m) : "v"(s[0]), "v"(s[1]), "v"(s[2]), "v"(s[3]), "v"(s[4]), "v"(s[5]), "v"(s[6]), "v"(s[7]), "v"(s[8]), "v"(s[9]),
          "v"(s[10]), "v"(s[11]), "v"(s[12]), "v"(s[13]), "v"(s[14]), "v"(s[15]));
    return m;
}
#define B2_TILE(sc, sn, kb, vb) do {                                                                            \
        const u4 kn0 = lds_ld(kb), kn1 = lds_ld((kb) + 1024), v0 = lds_ld(vb), v1 = lds_ld((vb) + 1024);         \
        float mx = max16(sc);                                                                                   \
        if (__builtin_expect(__any(mx > THR), 0)) {     \
            mx = swap32_partner_max(mx);                                                                        \
            const float dlt = fmaxf(mx, 0.f), al = ex2(-dlt);                                                   \
            m += dlt;                                                                                           \
            asm volatile("s_nop 7\n\ts_nop 3" : "+v"(o));                                                       \
            _Pragma("unroll") for (int r = 0; r < 16; ++r) { o[r] *= al; sc[r] -= dlt; negm[r] = -m; }           \
            asm volatile("s_nop 1" : "+v"(o), "+v"(negm), "+v"(sc));                                            \
        }                                                                                                       \
        MF32C(sn, kn0, q0, negm);                                                                               \
        u4 p0 = u4{pk(ex2(sc[0]), ex2(sc[1])), pk(ex2(sc[2]), ex2(sc[3])), pk(ex2(sc[4]), ex2(sc[5])), pk(ex2(sc[6]), ex2(sc[7]))}; \
        PINV(p0);                                                                                               \
        MF32(sn, kn1, q1);                                                                                      \
        MF32N(o, v0, p0);                                                                                       \
        u4 p1 = u4{pk(ex2(sc[8]), ex2(sc[9])), pk(ex2(sc[10]), ex2(sc[11])), pk(ex2(sc[12]), ex2(sc[13])), pk(ex2(sc[14]), ex2(sc[15]))}; \
        MF32N(o, v1, p1);                                                                                       \
    } while (0)
#define B2_FAST(sc, sn, kb, vb) do {                                                                            \
        const u4 kn0 = lds_ld(kb), kn1 = lds_ld((kb) + 1024), v0 = lds_ld(vb), v1 = lds_ld((vb) + 1024);         \
        SB();                                                                                                   \
        u4 p0 = u4{pk(ex2(sc[0]), ex2(sc[1])), pk(ex2(sc[2]), ex2(sc[3])), pk(ex2(sc[4]), ex2(sc[5])), pk(ex2(sc[6]), ex2(sc[7]))}; \
        PINV(p0);                                                                                               \
        SB();                                                                                                   \
        MF32C(sn, kn0, q0, negm);                                                                               \
        SB();                                                                                                   \
        float x8 = ex2(sc[8]), x9 = ex2(sc[9]), x10 = ex2(sc[10]), x11 = ex2(sc[11]);                            \
        asm volatile("" : "+v"(x8), "+v"(x9), "+v"(x10), "+v"(x11));                                            \
        SB();                                                                                                   \
        MF32(sn, kn1, q1);                                                                                      \
        SB();                                                                                                   \
        u4 p1 = u4{pk(x8, x9), pk(x10, x11), pk(ex2(sc[12]), ex2(sc[13])), pk(ex2(sc[14]), ex2(sc[15]))};        \
        PINV(p1);                                                                                               \
        SB();                                                                                                   \
        MF32N(o, v0, p0);                                                                                       \
        MF32(o, v1, p1);                                                                                        \
        SB();                                                                                                   \
    } while (0)
__global__ void __launch_bounds__(1024, 4) k_b2(char* __restrict__ ws) {
    const int tid = threadIdx.x, lane = tid & 63, w = __builtin_amdgcn_readfirstlane(tid >> 6);
    const int half = blockIdx.x & 1, qg = blockIdx.x >> 1, sub = w >> 3;
    const int qt = qg * 8 + (w & 7), h = qt >> 6, tt = qt & 63;
    const int l31 = lane & 31, hh = lane >> 5;
    const u16* qp = (const u16*)(ws + OFF_BQ) + ((size_t)h * SEQ + tt * 32 + l31) * 32 + 8 * hh;
    const float* knp = (const float*)(ws + OFF_BN) + 2 * lane;
    u4 q0, q1; u2 kn2;
    asm volatile("global_load_dwordx4 %0, %3, off\n\tglobal_load_dwordx4 %1, %3, off offset:32\n\tglobal_load_dwordx2 %2, %4, off"
                 : "=&v"(q0), "=&v"(q1), "=&v"(kn2) : "v"(qp), "v"(knp) : "memory");
    {
#pragma unroll
        for (int g = 0; g < 4; ++g)
#pragma unroll
            for (int e = 0; e < 2; ++e) {
                const int p = 2 * w + e, tile = 16 * (p >> 4) + 4 * g + ((p >> 2) & 3), part = p & 3;
                const unsigned off = (part >> 1) * 65536 + tile * 2048 + (part & 1) * 1024;
                const char* src = ws + ((part >> 1) ? OFF_BV : OFF_BK) + (size_t)half * 65536 + tile * 2048 + (part & 1) * 1024 + lane * 16;
                glds16(src, off);
            }
    }
    asm volatile("s_waitcnt vmcnt(6)" : "+v"(q0), "+v"(q1), "+v"(kn2) :: "memory");
    __builtin_amdgcn_s_barrier();
    asm volatile("" ::: "memory");
    float m;
    {
        float qq = 0;
#pragma unroll
        for (int e = 0; e < 4; ++e) { const unsigned a = q0[e], c = q1[e]; qq += bflo(a) * bflo(a) + bfhi(a) * bfhi(a) + bflo(c) * bflo(c) + bfhi(c) * bfhi(c); }
        qq = swap32_partner_add(qq);
        float kmax = fmaxf(__uint_as_float(kn2.x), __uint_as_float(kn2.y));
        kmax = fmaxf(kmax, __uint_as_float(__builtin_amdgcn_update_dpp(0u, __float_as_uint(kmax), 0x128, 0xf, 0xf, false)));
        kmax = fmaxf(kmax, __uint_as_float(__builtin_amdgcn_update_dpp(0u, __float_as_uint(kmax), 0x124, 0xf, 0xf, false)));
        kmax = fmaxf(kmax, __uint_as_float(__builtin_amdgcn_update_dpp(0u, __float_as_uint(kmax), 0x122, 0xf, 0xf, false)));
        kmax = fmaxf(kmax, __uint_as_float(__builtin_amdgcn_update_dpp(0u, __float_as_uint(kmax), 0x121, 0xf, 0xf, false)));
        kmax = swap16_partner_max(kmax); kmax = swap32_partner_max(kmax);
        m = 1.01f * __builtin_sqrtf(qq * kmax);
    }
    f16v o, negm, sA, sB;
#pragma unroll
    for (int r = 0; r < 16; ++r) { o[r] = 0.f; negm[r] = -m; }
    const unsigned lb = lane * 16 + sub * 32768;
    PINV(negm); PINV(o);
    asm volatile("s_nop 1" ::: "memory");
    { const u4 k0 = lds_ld(lb), k1 = lds_ld(lb + 1024); MF32C(sA, k0, q0, negm); MF32(sA, k1, q1); }
    asm volatile("s_nop 7\n\ts_nop 3" : "+v"(sA));
#pragma unroll
    for (int kt = 0; kt < 16; kt += 2) {
        if (kt == 2) { asm volatile("s_waitcnt vmcnt(4)" ::: "memory"); __builtin_amdgcn_s_barrier(); asm volatile("" ::: "memory"); }
        if (kt == 6) { asm volatile("s_waitcnt vmcnt(2)" ::: "memory"); __builtin_amdgcn_s_barrier(); asm volatile("" ::: "memory"); }
        if (kt == 10) { asm volatile("s_waitcnt vmcnt(0)" ::: "memory"); __builtin_amdgcn_s_barrier(); asm volatile("" ::: "memory"); }
        B2_FAST(sA, sB, lb + (kt + 1) * 2048, 65536 + lb + kt * 2048);
        B2_FAST(sB, sA, lb + ((kt + 2) & 15) * 2048, 65536 + lb + (kt + 1) * 2048);
    }
    asm volatile("s_nop 7\n\ts_nop 3" : "+v"(o), "+v"(sA));
    {
        auto sw = __builtin_amdgcn_permlane32_swap(__float_as_uint(o[12]), __float_as_uint(o[12]), false, false);
        constexpr float LMIN = 0x1p-80f;
        if (__builtin_expect(__any(!(__uint_as_float(sw[1]) >= LMIN)), 0)) {
#pragma unroll
            for (int r = 0; r < 16; ++r) { o[r] = 0.f; negm[r] = 0.f; }
            const u4 k0 = lds_ld(lb), k1 = lds_ld(lb + 1024);
            PINV(negm); PINV(o);
            asm volatile("s_nop 1" ::: "memory");
            MF32C(sA, k0, q0, negm); MF32(sA, k1, q1);
            asm volatile("s_nop 7\n\ts_nop 3" : "+v"(sA));
            m = swap32_partner_max(max16(sA));
#pragma unroll
            for (int r = 0; r < 16; ++r) { negm[r] = -m; sA[r] -= m; }
            asm volatile("s_nop 1" : "+v"(negm), "+v"(sA));
#pragma unroll 1
            for (int kt = 0; kt < 16; kt += 2) {
                B2_TILE(sA, sB, lb + (kt + 1) * 2048, 65536 + lb + kt * 2048);
                B2_TILE(sB, sA, lb + ((kt + 2) & 15) * 2048, 65536 + lb + (kt + 1) * 2048);
            }
            asm volatile("s_nop 7\n\ts_nop 3" : "+v"(o));
        }
    }
    __builtin_amdgcn_s_barrier();
    const unsigned mo = (w & 7) * 5120 + lane * 16;
    if (sub == 1) {
#pragma unroll
        for (int g = 0; g < 4; ++g) *(LAS f4*)(size_t)(mo + g * 1024) = f4{o[4 * g], o[4 * g + 1], o[4 * g + 2], o[4 * g + 3]};
        *(LAS float*)(size_t)((w & 7) * 5120 + 4096 + lane * 4) = m;
    }
    lds_barrier();
    if (sub == 0) {
        const float m1 = *(LAS const float*)(size_t)((w & 7) * 5120 + 4096 + lane * 4);
        const float M = fmaxf(m, m1), a0 = ex2(m - M), a1 = ex2(m1 - M);
#pragma unroll
        for (int g = 0; g < 4; ++g) {
            const f4 t = *(LAS const f4*)(size_t)(mo + g * 1024);
#pragma unroll
            for (int r = 0; r < 4; ++r) o[4 * g + r] = o[4 * g + r] * a0 + t[r] * a1;
        }
        const float lsum_own = o[12];
        auto sw = __builtin_amdgcn_permlane32_swap(__float_as_uint(lsum_own), __float_as_uint(lsum_own), false, false);
        const float l = __uint_as_float(sw[1]);
        const float inv = 1.0f / l;
        const int tok = tt * 32 + l31;
        u16* op = (u16*)(ws + OFF_BO) + ((size_t)half * SEQ + tok) * DM + h * 28 + 4 * hh;
#pragma unroll
        for (int g = 0; g < 4; ++g) {
            if (g < 3 || hh == 0) *(u2*)(op + 8 * g) = u2{pk(o[4 * g] * inv, o[4 * g + 1] * inv), pk(o[4 * g + 2] * inv, o[4 * g + 3] * inv)};
        }
        if (hh == 0) { typedef float f2 __attribute__((ext_vector_type(2))); *(f2*)(ws + OFF_BS + (((size_t)half * 16 + h) * SEQ + tok) * 8) = f2{M, l}; }
    }
}

template <int NT>
DEV void b3f_wave(char* __restrict__ ws, u16* __restrict__ out, int tile, int T0, u16* xb) {
    const int tid = threadIdx.x, lane = tid & 63;
    typedef float f2 __attribute__((ext_vector_type(2)));
    const int l15 = lane & 15, kg = lane >> 4;
    const float* x32 = (const float*)(ws + OFF_X32);
    const int tok = tile * 16 + l15;
    u4 oa[4], ob[4]; f2 sa[4][2], sb[4][2];
#pragma unroll
    for (int k = 0; k < 4; ++k) {
        const int i = tid + 256 * k < 896 ? tid + 256 * k : 895, r = i / 56, c8 = (i % 56) * 8, tk = tile * 16 + r;
        oa[k] = *(const u4*)((const u16*)(ws + OFF_BO) + (size_t)tk * DM + c8);
        ob[k] = *(const u4*)((const u16*)(ws + OFF_BO) + ((size_t)SEQ + tk) * DM + c8);
#pragma unroll
        for (int e = 0; e < 2; ++e) {
            const int h = (c8 + 4 * e) / 28;
            sa[k][e] = *(const f2*)(ws + OFF_BS + ((size_t)h * SEQ + tk) * 8); sb[k][e] = *(const f2*)(ws + OFF_BS + ((size_t)(16 + h) * SEQ + tk) * 8);
        }
    }
    u4 wf[NT][14];
#pragma unroll
    for (int ti = 0; ti < NT; ++ti)
#pragma unroll
        for (int ks = 0; ks < 14; ++ks) wf[ti][ks] = *(const u4*)(ws + OFF_WOF + ((size_t)(T0 + ti) * 14 + ks) * 1024 + lane * 16);
    f4 xv[NT];
#pragma unroll
    for (int ti = 0; ti < NT; ++ti) xv[ti] = *(const f4*)(x32 + (size_t)tok * DM + 16 * (T0 + ti) + 4 * kg);
#pragma unroll
    for (int k = 0; k < 4; ++k) {
        const int i = tid + 256 * k;
        if (i < 896) {
            const int r = i / 56, c8 = (i % 56) * 8;
            float wa[2], wb[2];
#pragma unroll
            for (int e = 0; e < 2; ++e) {
                const float M = fmaxf(sa[k][e][0], sb[k][e][0]);
                const float la = sa[k][e][1] * ex2(sa[k][e][0] - M), lb = sb[k][e][1] * ex2(sb[k][e][0] - M);
                const float inv = 1.0f / (la + lb);
                wa[e] = la * inv; wb[e] = lb * inv;
            }
            const u4 a = oa[k], b = ob[k];
            u4 o;
            o.x = pk(wa[0] * bflo(a.x) + wb[0] * bflo(b.x), wa[0] * bfhi(a.x) + wb[0] * bfhi(b.x));
            o.y = pk(wa[0] * bflo(a.y) + wb[0] * bflo(b.y), wa[0] * bfhi(a.y) + wb[0] * bfhi(b.y));
            o.z = pk(wa[1] * bflo(a.z) + wb[1] * bflo(b.z), wa[1] * bfhi(a.z) + wb[1] * bfhi(b.z));
            o.w = pk(wa[1] * bflo(a.w) + wb[1] * bflo(b.w), wa[1] * bfhi(a.w) + wb[1] * bfhi(b.w));
            *(u4*)(xb + r * 456 + c8) = o;
        }
    }
    __syncthreads();
    bf8 xf[14];
#pragma unroll
    for (int ks = 0; ks < 14; ++ks) xf[ks] = asbf8(*(const u4*)(xb + l15 * 456 + 32 * ks + 8 * kg));
#pragma unroll
    for (int ti = 0; ti < NT; ++ti) {
        f4 d = {0, 0, 0, 0};
#pragma unroll
        for (int ks = 0; ks < 14; ++ks) d = mfma16(asbf8(wf[ti][ks]), xf[ks], d);
        const f4 v = xv[ti] + d;
        *(u2*)(out + (size_t)tok * DM + 16 * (T0 + ti) + 4 * kg) = u2{pk(v[0], v[1]), pk(v[2], v[3])};
    }
}
__global__ void __launch_bounds__(256, 1) k_b3(char* __restrict__ ws, u16* __restrict__ out) {
    __shared__ __attribute__((aligned(16))) u16 xb[16 * 456];
    const int tile = blockIdx.x >> 1, nh = blockIdx.x & 1, w = __builtin_amdgcn_readfirstlane(threadIdx.x >> 6);
    if (w < 2) b3f_wave<4>(ws, out, tile, nh * 14 + 4 * w, xb); else b3f_wave<3>(ws, out, tile, nh * 14 + 8 + 3 * (w - 2), xb);
}

extern "C" void kernel_launch(void* const* d_in, const int* in_sizes, int n_in, void* d_out, int out_size, void* d_ws, size_t ws_size,
                              hipStream_t stream) {
    const u16* x = (const u16*)d_in[0];
    const u16* mem0 = (const u16*)d_in[1];
    const u16 *Wq = (const u16*)d_in[2], *Wk = (const u16*)d_in[3], *Wv = (const u16*)d_in[4], *Wo = (const u16*)d_in[5];
    const u16 *wq = (const u16*)d_in[8], *wk = (const u16*)d_in[9], *wv = (const u16*)d_in[10], *wo = (const u16*)d_in[11];
    char* ws = (char*)(((uintptr_t)d_ws + (2 * MB - 1)) & ~(uintptr_t)(2 * MB - 1));
    PrepArgs pa;
    pa.x = x; pa.mem0 = mem0; pa.Wq = Wq; pa.Wk = Wk; pa.Wv = Wv; pa.Wo = Wo; pa.wq = wq; pa.wk = wk; pa.wv = wv; pa.wo = wo; pa.ws = ws;
    k_prep<<<512, 256, 65536, stream>>>(pa);
    (void)hipFuncSetAttribute((const void*)k_rec, hipFuncAttributeMaxDynamicSharedMemorySize, 96 * 1024);
    (void)hipFuncSetAttribute((const void*)k_b2, hipFuncAttributeMaxDynamicSharedMemorySize, 128 * 1024);
    for (int layer = 0; layer < 2; ++layer) {
        RecArgs ra;
        ra.ws = ws; ra.x0 = x; ra.mem0 = mem0; ra.layer = layer; ra.dA = 16; ra.dB1 = 6; ra.dB2 = 10; ra.dC = 13; ra.pad = 0;
        k_rec<<<256, 256, 96 * 1024, stream>>>(ra);
        k_b2<<<256, 1024, 128 * 1024, stream>>>(ws);
    }
    k_b3<<<256, 256, 0, stream>>>(ws, (u16*)d_out);
}
```
